```python
import torch

def _softmax(x, axis=-1):
    return torch.softmax(x, dim=axis)
def _gelu(x):
    return torch.nn.functional.gelu(x, approximate="tanh")
def _silu(x):
    return torch.nn.functional.silu(x)
def _sigmoid(x):
    return torch.sigmoid(x)
def _layernorm(x, axis=-1, eps=1e-5):
    m = x.mean(dim=axis, keepdim=True)
    v = x.var(dim=axis, keepdim=True, unbiased=False)
    return (x - m) / torch.sqrt(v + eps)
def _scatter_add(tgt, idx, src, axis=0):
    return tgt.clone().index_add_(axis, idx, src)
def _segment_sum(src, seg, n):
    out = torch.zeros((n,) + src.shape[1:], dtype=src.dtype)
    return out.index_add_(0, seg, src)
def _pad(x, pw):
    flat = tuple(v for pair in reversed(pw) for v in pair)
    return torch.nn.functional.pad(x, flat)

kernel_name = 'recurrent_scan__spectral_fnet__dense_activation_gelu__prompt'

def setup_inputs():

    _w = (lambda a: (lambda t: t.to(torch.bfloat16) if t.is_floating_point() else t)(torch.as_tensor(a)))
    def _A_setup():
        B, S, D = 1, 2048, 1024
        ns = torch.arange(S)[:, None] * torch.arange(S)[None, :]
        nd = torch.arange(D)[:, None] * torch.arange(D)[None, :]
        Ws_re = torch.cos(-2.0 * torch.pi * ns / S).to(float)
        Ws_im = torch.sin(-2.0 * torch.pi * ns / S).to(float)
        Wd_re = torch.cos(-2.0 * torch.pi * nd / D).to(float)
        Wd_im = torch.sin(-2.0 * torch.pi * nd / D).to(float)
        return {
            "x": _w((lambda *s: torch.randn(*s, dtype=torch.bfloat16) * 0.05)(B, S, D)),
            "dft_seq_re": _w(Ws_re),
            "dft_seq_im": _w(Ws_im),
            "dft_hid_re": _w(Wd_re),
            "dft_hid_im": _w(Wd_im),
        }
    _A_in = _A_setup()

    def _B_setup():
        B, S, D, F = 1, 2048, 1024, 3584
        return {
            "x": _w((lambda *s: torch.randn(*s, dtype=torch.bfloat16) * 0.05)(B, S, D)),
            "w1": _w((lambda *s: torch.randn(*s, dtype=torch.bfloat16) * 0.2)(D, F) * (D ** -0.5)),
            "b1": _w((lambda *s: torch.randn(*s, dtype=torch.bfloat16) * 3.2)(F) * 0.01),
            "w2": _w((lambda *s: torch.randn(*s, dtype=torch.bfloat16) * 0.05)(F, D) * (F ** -0.5)),
            "b2": _w((lambda *s: torch.randn(*s, dtype=torch.bfloat16) * 0.1)(D) * 0.01),
        }
    _B_in = _B_setup()

    return {
        "A_x": _A_in["x"],
        "A_dft_seq_re": _A_in["dft_seq_re"],
        "A_dft_seq_im": _A_in["dft_seq_im"],
        "A_dft_hid_re": _A_in["dft_hid_re"],
        "A_dft_hid_im": _A_in["dft_hid_im"],
        "B_x": _B_in["x"],
        "B_w1": _B_in["w1"],
        "B_b1": _B_in["b1"],
        "B_w2": _B_in["w2"],
        "B_b2": _B_in["b2"],
    }

def reference(A_x, A_dft_seq_re, A_dft_seq_im, A_dft_hid_re, A_dft_hid_im, B_x, B_w1, B_b1, B_w2, B_b2):
    def _A_ref(x, dft_seq_re, dft_seq_im, dft_hid_re, dft_hid_im):
        a = torch.einsum("bsd,ed->bse", x, dft_hid_re)
        b = torch.einsum("bsd,ed->bse", x, dft_hid_im)
        y = torch.einsum("ts,bse->bte", dft_seq_re, a) - torch.einsum("ts,bse->bte", dft_seq_im, b)
        return y

    def _B_ref(x, w1, b1, w2, b2):
        h = torch.einsum("bsd,df->bsf", x, w1) + b1
        h = _gelu(h)
        return torch.einsum("bsf,fd->bsd", h, w2) + b2

    x = A_x
    xn = _layernorm(x, axis=-1)
    s = _A_ref(xn, A_dft_seq_re, A_dft_seq_im, A_dft_hid_re, A_dft_hid_im)
    g = _silu(_B_ref(xn, B_w1, B_b1, B_w2, B_b2))
    return x + s * g
```

```cpp
#include <hip/hip_runtime.h>
#include <hip/hip_bf16.h>
#include <type_traits>

#define LAS __attribute__((address_space(3)))
#define DI __device__ __forceinline__
typedef unsigned short u16;
typedef short bf16x8 __attribute__((ext_vector_type(8)));
typedef short bf16x4 __attribute__((ext_vector_type(4)));
typedef float f32x4 __attribute__((ext_vector_type(4)));
typedef float f32x2 __attribute__((ext_vector_type(2)));
typedef unsigned u32x4 __attribute__((ext_vector_type(4)));
typedef unsigned u32x2 __attribute__((ext_vector_type(2)));

constexpr int S_ = 2048, D_ = 1024, F_ = 3584, ZK = 2 * D_ + F_;
constexpr int ZKB = ZK / 32;
constexpr size_t WS_XA = 0;
constexpr size_t WS_XB = WS_XA + (size_t)S_ * D_ * 2;
constexpr size_t WS_Z = WS_XB + (size_t)S_ * D_ * 2;
constexpr size_t WS_P = WS_Z + (size_t)S_ * ZK * 2;
constexpr unsigned OOB = 0x40000000u;

template <int N> DI void wait_vm() { asm volatile("s_waitcnt vmcnt(%0)" ::"n"(N) : "memory"); }
DI void wait_lgkm0() { asm volatile("s_waitcnt lgkmcnt(0)" ::: "memory"); }
DI void bar() { asm volatile("s_barrier" ::: "memory"); }
DI unsigned cvt_pk_bf16(float lo, float hi) {
  typedef __bf16 bf16x2 __attribute__((ext_vector_type(2)));
  f32x2 v = {lo, hi}; bf16x2 b = __builtin_convertvector(v, bf16x2); return __builtin_bit_cast(unsigned, b);
}
DI float bf_lo(unsigned w) { return __uint_as_float(w << 16); }
DI float bf_hi(unsigned w) { return __uint_as_float(w & 0xffff0000u); }
DI float rbf(float v) { return bf_lo(cvt_pk_bf16(v, v)); }
template <int I, int N, class F> DI void static_for(F&& f) { if constexpr (I < N) { f(std::integral_constant<int, I>{}); static_for<I + 1, N>(f); } }

DI void mfma16(f32x4& acc, const bf16x8& a, const bf16x8& b) { asm volatile("v_mfma_f32_16x16x32_bf16 %0, %1, %2, %0" : "+a"(acc) : "v"(a), "v"(b)); }
template <int OFF> DI void ds_r128(bf16x8& d, unsigned addr) { asm volatile("ds_read_b128 %0, %1 offset:%2" : "=v"(d) : "v"(addr), "n"(OFF) : "memory"); }
template <int OFF> DI void ds_rtr(bf16x4& d, unsigned addr) { asm volatile("ds_read_b64_tr_b16 %0, %1 offset:%2" : "=v"(d) : "v"(addr), "n"(OFF) : "memory"); }
DI void set_m0(unsigned v) { asm volatile("s_mov_b32 m0, %0" ::"s"(v) : "memory"); }
template <int OFF> DI void dma16(int voff, __amdgpu_buffer_rsrc_t r, unsigned soff) {
  asm volatile("buffer_load_dwordx4 %0, %1, %2 offen offset:%3 lds" ::"v"(voff), "s"(r), "s"(soff), "n"(OFF) : "memory");
}
DI void pin8(bf16x8& v) { asm volatile("" : "+v"(v)); }
DI void pin4(bf16x4& v) { asm volatile("" : "+v"(v)); }
DI __amdgpu_buffer_rsrc_t mkrsrc(const void* p, unsigned bytes) { return __builtin_amdgcn_make_buffer_rsrc((void*)p, 0, bytes, 0x00020000); }

enum { ROWK = 0, FRAG = 1, NMAJ = 2 };

template <int R> struct Stream {
  static constexpr int P = R / 32, NV = (R == 224) ? 7 : 4, BYTES = R * 128;
  __amdgpu_buffer_rsrc_t rs;
  int voff[NV];
  unsigned s0, gs, ks;
  DI void init_rowk(const void* p, unsigned bytes, int ld, int r0, int k0, int wid, int lane) {
    rs = mkrsrc(p, bytes);
#pragma unroll
    for (int j = 0; j < 4; ++j) voff[j] = ((lane >> 3) + 8 * j) * ld * 2 + (((lane & 7) ^ ((4 * (j & 1) + (lane >> 4)) & 7)) * 16) - j * 1024;
    s0 = (unsigned)((r0 + 32 * wid) * ld + k0) * 2u; gs = (unsigned)(128 * ld) * 2u; ks = 128u;
  }
  DI void init_frag(const void* p, unsigned bytes, int kbn, int r0, int k0, int wid, int lane) {
    rs = mkrsrc(p, bytes);
#pragma unroll
    for (int j = 0; j < 4; ++j) voff[j] = lane * 16 + (j >> 1) * (kbn * 1024 - 2048);
    s0 = (unsigned)((r0 / 16 + 2 * wid) * kbn + k0 / 32) * 1024u; gs = (unsigned)(8 * kbn) * 1024u; ks = 2048u;
  }
  DI void init_nmaj(const void* p, unsigned bytes, int ld, int n0, int k0, int wid, int lane) {
    rs = mkrsrc(p, bytes);
    if constexpr (R == 224) {
#pragma unroll
      for (int j = 0; j < 7; ++j) { const int L = (wid * 7 + j) * 64 + lane, kk = L / 28, pos = L % 28, ch = pos ^ (((kk >> 3) & 1) << 1);
        voff[j] = kk * ld * 2 + ch * 16 - (j & 3) * 1024; }
      s0 = (unsigned)(k0 * ld + n0) * 2u; gs = 0;
    } else {
      constexpr int CPR = R / 8, RP = 64 / CPR;
#pragma unroll
      for (int j = 0; j < 4; ++j) { const int kk = RP * (4 * wid + j) + lane / CPR, g = (kk & 3) | (((kk >> 3) & 1) << 2);
        voff[j] = (RP * j + lane / CPR) * ld * 2 + (((lane % CPR) ^ (g << 1)) * 16) - j * 1024; }
      s0 = (unsigned)((k0 + 4 * RP * wid) * ld + n0) * 2u; gs = (unsigned)(16 * RP * ld) * 2u;
    }
    ks = (unsigned)(64 * ld) * 2u;
  }
  template <int I> DI unsigned ldsoff(int wid) const { if constexpr (R == 224) return (unsigned)(wid * 7 + (I & ~3)) * 1024u; else return (unsigned)(4 * (wid + 4 * (I / 4))) * 1024u; }
  template <int I> DI void issue(unsigned koff) const {
    if constexpr (R == 224) dma16<(I & 3) * 1024>(voff[I], rs, s0 + koff); else dma16<(I % 4) * 1024>(voff[I % 4], rs, s0 + (I / 4) * gs + koff);
  }
};

template <int KIND, int R, int T> struct Reader {
  static constexpr int NA = KIND == ROWK ? 2 : (KIND == FRAG ? 1 : (R == 224 ? 2 : T));
  static constexpr int NI = KIND == NMAJ ? 2 * T : T;
  unsigned a[NA];
  DI void init(unsigned base, int t0, int lane) {
    const int fr = lane & 15, fq = lane >> 4;
    if constexpr (KIND == ROWK) { a[0] = base + (t0 * 16 + fr) * 128 + ((fq ^ (fr >> 1)) * 16); a[1] = base + (t0 * 16 + fr) * 128 + (((4 + fq) ^ (fr >> 1)) * 16); }
    else if constexpr (KIND == FRAG) a[0] = base + t0 * 2048 + lane * 16;
    else {
      const int q = fr >> 2, p = lane & 3;
      if constexpr (R == 224) {
        a[0] = base + (8 * fq + q) * 448 + 32 * (t0 ^ (fq & 1)) + 8 * p; a[1] = base + (8 * fq + q) * 448 + 32 * ((t0 + 1) ^ (fq & 1)) + 8 * p;
      } else {
        const int gk = q | ((fq & 1) << 2);
#pragma unroll
        for (int n = 0; n < T; ++n) a[n] = base + (8 * fq + q) * (R * 2) + 32 * ((t0 + n) ^ gk) + 8 * p;
      }
    }
  }
};
template <int T> struct FragSet { bf16x8 f[T]; bf16x4 lo[T], hi[T]; };
template <int KIND, int R, int T, int KS, int I> DI void rd1(const Reader<KIND, R, T>& r, FragSet<T>& d, unsigned so) {
  if constexpr (KIND == ROWK) ds_r128<I * 2048>(d.f[I], r.a[KS] + so);
  else if constexpr (KIND == FRAG) ds_r128<I * 2048 + KS * 1024>(d.f[I], r.a[0] + so);
  else { constexpr int n = I / 2, h = I % 2, ko = (32 * KS + 4 * h) * (R * 2);
    if constexpr (R == 224) { if constexpr (h == 0) ds_rtr<ko + 32 * (n & ~1)>(d.lo[n], r.a[n & 1] + so); else ds_rtr<ko + 32 * (n & ~1)>(d.hi[n], r.a[n & 1] + so); }
    else { if constexpr (h == 0) ds_rtr<ko>(d.lo[n], r.a[n] + so); else ds_rtr<ko>(d.hi[n], r.a[n] + so); } }
}
template <int KIND, int T> DI void land1(FragSet<T>& d) {
#pragma unroll
  for (int i = 0; i < T; ++i) {
    if constexpr (KIND == NMAJ) { pin4(d.lo[i]); pin4(d.hi[i]); d.f[i] = __builtin_shufflevector(d.lo[i], d.hi[i], 0, 1, 2, 3, 4, 5, 6, 7); }
    else pin8(d.f[i]);
  }
}

template <int BM_, int BN_, int NS_> struct Geo {
  static constexpr int BM = BM_, BN = BN_, NS = NS_, MT = BM / 32, NT = BN / 32;
  static constexpr int PA = BM / 32, PB = BN / 32, ND = PA + PB, STAGE = (BM + BN) * 128, H = (NS == 2) ? ND : ND / 2;
};
template <class G> struct Pipe {
  Stream<G::BM> A; Stream<G::BN> B;
  unsigned lds; int wid;
  static constexpr bool gstart(int i) { return i < G::PA ? (i % 4 == 0) : ((i - G::PA) % 4 == 0); }
  template <int I> DI void m0(unsigned so) const {
    if constexpr (I < G::PA) set_m0(lds + so + A.template ldsoff<I>(wid)); else set_m0(lds + so + G::BM * 128 + B.template ldsoff<I - G::PA>(wid));
  }
  template <int I> DI void issue(unsigned ka, unsigned kb) const { if constexpr (I < G::PA) A.template issue<I>(ka); else B.template issue<I - G::PA>(kb); }
};

template <class G, int KA, int KB, int RKS, int D0, int D1>
DI void phase(const Pipe<G>& p, const Reader<KA, G::BM, G::MT>& ra, const Reader<KB, G::BN, G::NT>& rb, f32x4 (&acc)[G::MT][G::NT],
              const FragSet<G::MT>& ca, const FragSet<G::NT>& cb, FragSet<G::MT>& na, FragSet<G::NT>& nb, unsigned rso, unsigned ka, unsigned kb, unsigned dso) {
  constexpr int NM = G::MT * G::NT, NIA = Reader<KA, G::BM, G::MT>::NI, NIB = Reader<KB, G::BN, G::NT>::NI, NR = NIA + NIB, NDP = D1 - D0, RSPAN = (NM * 3) / 4;
  static_for<0, NM>([&](auto I) {
    constexpr int i = I.value, m = i / G::NT, n = i % G::NT;
    if constexpr (NDP > 0) static_for<0, NDP>([&](auto Dd) { constexpr int d = Dd.value;
      if constexpr ((d * (NM - 2)) / NDP + 1 == i && (d == 0 || Pipe<G>::gstart(D0 + d))) p.template m0<D0 + d>(dso); });
    mfma16(acc[m][n], cb.f[n], ca.f[m]);
    if constexpr (NDP > 0) static_for<0, NDP>([&](auto Dd) { constexpr int d = Dd.value;
      if constexpr ((d * (NM - 2)) / NDP + 1 == i) p.template issue<D0 + d>(ka, kb); });
    static_for<0, NR>([&](auto Rr) { constexpr int r = Rr.value;
      if constexpr ((r * RSPAN) / NR == i) {
        if constexpr (r == 0) rd1<KA, G::BM, G::MT, RKS, 0>(ra, na, rso);
        else if constexpr (r <= NIB) rd1<KB, G::BN, G::NT, RKS, r - 1>(rb, nb, rso);
        else rd1<KA, G::BM, G::MT, RKS, r - NIB>(ra, na, rso);
      } });
  });
  wait_lgkm0(); land1<KA>(na); land1<KB>(nb);
}

template <class G, int KA, int KB>
DI void gemm_loop(const Pipe<G>& p, int lane, f32x4 (&acc)[G::MT][G::NT], int nt) {
  constexpr int NS = G::NS, ND = G::ND, H = G::H;
  Reader<KA, G::BM, G::MT> ra; Reader<KB, G::BN, G::NT> rb;
  ra.init(p.lds, (p.wid >> 1) * G::MT, lane); rb.init(p.lds + G::BM * 128, (p.wid & 1) * G::NT, lane);
  FragSet<G::MT> xa, ya; FragSet<G::NT> xb, yb;
  asm volatile("s_nop 4" ::: "memory");
  static_for<0, NS>([&](auto Ss) { static_for<0, (Ss.value == NS - 1 ? H : ND)>([&](auto Dd) {
    if constexpr (Pipe<G>::gstart(Dd.value)) { p.template m0<Dd.value>(Ss.value * G::STAGE); asm volatile("s_nop 0"); }
    p.template issue<Dd.value>(Ss.value * p.A.ks, Ss.value * p.B.ks); }); });
  wait_vm<(NS - 2) * ND + H>();
  bar();
  static_for<0, Reader<KA, G::BM, G::MT>::NI>([&](auto I) { rd1<KA, G::BM, G::MT, 0, I.value>(ra, xa, 0); });
  static_for<0, Reader<KB, G::BN, G::NT>::NI>([&](auto I) { rd1<KB, G::BN, G::NT, 0, I.value>(rb, xb, 0); });
  wait_lgkm0(); land1<KA>(xa); land1<KB>(xb);
  asm volatile("s_nop 1");
  unsigned so = 0, sp = (NS - 1) * G::STAGE;
  for (int t = 0; t < nt; ++t) {
    unsigned sn = so + G::STAGE; if (sn == NS * G::STAGE) sn = 0;
    const int ta = t + NS - 1, tb = t + NS;
    const unsigned kaa = ta < nt ? ta * p.A.ks : OOB, kba = ta < nt ? ta * p.B.ks : OOB, kab = tb < nt ? tb * p.A.ks : OOB, kbb = tb < nt ? tb * p.B.ks : OOB;
    phase<G, KA, KB, 1, H, ND>(p, ra, rb, acc, xa, xb, ya, yb, so, kaa, kba, sp);
    wait_vm<(NS - 2) * ND>();
    bar();
    phase<G, KA, KB, 0, 0, H>(p, ra, rb, acc, ya, yb, xa, xb, sn, kab, kbb, so);
    sp = so; so = sn;
  }
  asm volatile("s_nop 7\n\ts_nop 7\n\ts_waitcnt vmcnt(0)" ::: "memory");
}

__global__ __launch_bounds__(128) void k_ln(const u16* __restrict__ x, unsigned char* __restrict__ ws) {
  __shared__ float red[2][2][8];
  const int tid = threadIdx.x, lane = tid & 63, w = tid >> 6, r0 = blockIdx.x * 8;
  u32x4 raw[8];
#pragma unroll
  for (int r = 0; r < 8; ++r) raw[r] = *(const u32x4*)(x + (size_t)(r0 + r) * D_ + tid * 8);
  float v[8][8];
#pragma unroll
  for (int r = 0; r < 8; ++r)
#pragma unroll
    for (int j = 0; j < 4; ++j) { v[r][2 * j] = bf_lo(raw[r][j]); v[r][2 * j + 1] = bf_hi(raw[r][j]); }
  float mean[8], den[8];
#pragma unroll
  for (int pass = 0; pass < 2; ++pass) {
    float s[8];
#pragma unroll
    for (int r = 0; r < 8; ++r) { float a = 0.f;
#pragma unroll
      for (int j = 0; j < 8; ++j) { if (pass == 0) a += v[r][j]; else { const float dd = v[r][j] - mean[r]; a += dd * dd; } }
      s[r] = a; }
    float t4[4], t2[2], t1;
#pragma unroll
    for (int i = 0; i < 4; ++i) { const bool up = lane & 1; const float keep = up ? s[4 + i] : s[i], send = up ? s[i] : s[4 + i]; t4[i] = keep + __shfl_xor(send, 1); }
#pragma unroll
    for (int i = 0; i < 2; ++i) { const bool up = lane & 2; const float keep = up ? t4[2 + i] : t4[i], send = up ? t4[i] : t4[2 + i]; t2[i] = keep + __shfl_xor(send, 2); }
    { const bool up = lane & 4; const float keep = up ? t2[1] : t2[0], send = up ? t2[0] : t2[1]; t1 = keep + __shfl_xor(send, 4); }
    t1 += __shfl_xor(t1, 8); t1 += __shfl_xor(t1, 16); t1 += __shfl_xor(t1, 32);
    if (lane < 8) red[pass][w][(lane & 1) * 4 + ((lane >> 1) & 1) * 2 + ((lane >> 2) & 1)] = t1;
    __syncthreads();
#pragma unroll
    for (int r = 0; r < 8; ++r) { const float tot = (red[pass][0][r] + red[pass][1][r]) * (1.0f / D_);
      if (pass == 0) mean[r] = tot; else den[r] = 1.0f / rbf(sqrtf(rbf(rbf(tot) + 1e-5f))); }
  }
  unsigned o[8][4];
#pragma unroll
  for (int r = 0; r < 8; ++r) { const float mb = rbf(mean[r]);
#pragma unroll
    for (int j = 0; j < 4; ++j) o[r][j] = cvt_pk_bf16(rbf(v[r][2 * j] - mb) * den[r], rbf(v[r][2 * j + 1] - mb) * den[r]); }
  unsigned char* xa = ws + WS_XA + ((size_t)(r0 >> 4) * 32 + (tid >> 2)) * 1024 + (tid & 3) * 256 + (r0 & 15) * 16;
#pragma unroll
  for (int r = 0; r < 8; ++r) *(u32x4*)(xa + r * 16) = (u32x4){o[r][0], o[r][1], o[r][2], o[r][3]};
  unsigned char* xb = ws + WS_XB + ((size_t)(tid >> 1) * 64 + (r0 >> 5)) * 1024 + ((r0 & 31) >> 3) * 256 + (tid & 1) * 128;
#pragma unroll
  for (int j = 0; j < 4; ++j) {
    u32x4 lo, hi;
#pragma unroll
    for (int rr = 0; rr < 4; ++rr) { lo[rr] = (o[2 * rr][j] & 0xffffu) | (o[2 * rr + 1][j] << 16); hi[rr] = (o[2 * rr][j] >> 16) | (o[2 * rr + 1][j] & 0xffff0000u); }
    *(u32x4*)(xb + (2 * j) * 16) = lo; *(u32x4*)(xb + (2 * j + 1) * 16) = hi;
  }
}

DI float gelu_tanh(float x) {
  const float k0 = -2.0f * 0.7978845608028654f * 1.4426950408889634f, k1 = k0 * 0.044715f;
  const float e = __builtin_amdgcn_exp2f(x * (k1 * x * x + k0));
  return x * __builtin_amdgcn_rcpf(1.0f + e);
}
DI float silu(float x) { return x * __builtin_amdgcn_rcpf(1.0f + __builtin_amdgcn_exp2f(-1.4426950408889634f * x)); }

typedef Geo<256, 128, 3> GU;
typedef Geo<256, 224, 2> GH;
typedef Geo<128, 128, 4> G2;
constexpr int LDS1 = GU::NS * GU::STAGE;
constexpr int LDS2 = G2::NS * G2::STAGE;

DI void store_pair(unsigned char* blk, const f32x4& a, const f32x4& b, int fr, int fq) {
  const unsigned a0 = cvt_pk_bf16(a[0], a[1]), a1 = cvt_pk_bf16(a[2], a[3]), b0 = cvt_pk_bf16(b[0], b[1]), b1 = cvt_pk_bf16(b[2], b[3]);
  const auto r0 = __builtin_amdgcn_permlane16_swap(a0, b0, false, false);
  const auto r1 = __builtin_amdgcn_permlane16_swap(a1, b1, false, false);
  *(u32x4*)(blk + ((fq & 1) * 2 + (fq >> 1)) * 256 + fr * 16) = (u32x4){r0[0], r1[0], r0[1], r1[1]};
}

__global__ __launch_bounds__(256, 1) void k_stage1(const u16* __restrict__ ws_re, const u16* __restrict__ ws_im, const u16* __restrict__ w1, const u16* __restrict__ b1, unsigned char* __restrict__ ws) {
  extern __shared__ __attribute__((aligned(16))) unsigned char shm[];
  const int wid = __builtin_amdgcn_readfirstlane(threadIdx.x >> 6), lane = threadIdx.x & 63, fr = lane & 15, fq = lane >> 4, wm = wid >> 1, wn = wid & 1;
  const int xcd = blockIdx.x & 7, slot = blockIdx.x >> 3;
  unsigned char* Z = ws + WS_Z;
  if (slot < 16) {
    const int pm = (xcd >> 1) * 4 + (slot & 3), pn = (xcd & 1) * 4 + (slot >> 2), im = pm >> 3, row0 = (pm & 7) * 256;
    Pipe<GU> p; p.lds = (unsigned)(size_t)(LAS unsigned char*)shm; p.wid = wid;
    p.A.init_rowk(im ? ws_im : ws_re, S_ * S_ * 2, S_, row0, 0, wid, lane);
    p.B.init_frag(ws + WS_XB, S_ * D_ * 2, 64, pn * 128, 0, wid, lane);
    f32x4 acc[GU::MT][GU::NT];
#pragma unroll
    for (int m = 0; m < GU::MT; ++m)
#pragma unroll
      for (int n = 0; n < GU::NT; ++n) acc[m][n] = (f32x4){0.f, 0.f, 0.f, 0.f};
    gemm_loop<GU, ROWK, FRAG>(p, lane, acc, S_ / 64);
    const float sg = im ? -1.f : 1.f;
    unsigned char* zb = Z + ((size_t)(row0 / 16 + wm * 8) * ZKB + (im * 1024 + pn * 128 + wn * 64) / 32) * 1024;
#pragma unroll
    for (int m = 0; m < GU::MT; ++m)
#pragma unroll
      for (int n = 0; n < GU::NT; n += 2) store_pair(zb + ((size_t)m * ZKB + n / 2) * 1024, acc[m][n] * sg, acc[m][n + 1] * sg, fr, fq);
  } else {
    const int s2 = slot - 16, pm = (xcd & 1) * 4 + (s2 & 3), pn = (xcd >> 1) * 4 + (s2 >> 2);
    Pipe<GH> p; p.lds = (unsigned)(size_t)(LAS unsigned char*)shm; p.wid = wid;
    p.A.init_frag(ws + WS_XA, S_ * D_ * 2, 32, pm * 256, 0, wid, lane);
    p.B.init_nmaj(w1, D_ * F_ * 2, F_, pn * 224, 0, wid, lane);
    const int c0 = pn * 224 + wn * 112;
    u32x2 bias[GH::NT];
#pragma unroll
    for (int n = 0; n < GH::NT; ++n) bias[n] = *(const u32x2*)(b1 + c0 + n * 16 + fq * 4);
    f32x4 acc[GH::MT][GH::NT];
#pragma unroll
    for (int m = 0; m < GH::MT; ++m)
#pragma unroll
      for (int n = 0; n < GH::NT; ++n) acc[m][n] = (f32x4){0.f, 0.f, 0.f, 0.f};
    gemm_loop<GH, FRAG, NMAJ>(p, lane, acc, D_ / 64);
    f32x4 bv[GH::NT];
#pragma unroll
    for (int n = 0; n < GH::NT; ++n) bv[n] = (f32x4){bf_lo(bias[n][0]), bf_hi(bias[n][0]), bf_lo(bias[n][1]), bf_hi(bias[n][1])};
    unsigned char* zb = Z + ((size_t)(pm * 16 + wm * 8) * ZKB + (2048 + pn * 224) / 32 + wn * 4) * 1024;
    unsigned char* zs = Z + ((size_t)(pm * 16 + wm * 8) * ZKB + (2048 + pn * 224) / 32 + 3) * 1024 + (wn * 2 + (fq >> 1)) * 256 + fr * 16 + (fq & 1) * 8;
#pragma unroll
    for (int m = 0; m < GH::MT; ++m) {
      f32x4 g[GH::NT];
#pragma unroll
      for (int n = 0; n < GH::NT; ++n) { const f32x4 t = acc[m][n] + bv[n]; g[n] = (f32x4){gelu_tanh(t[0]), gelu_tanh(t[1]), gelu_tanh(t[2]), gelu_tanh(t[3])}; }
      if (wn == 0) {
#pragma unroll
        for (int n = 0; n < 6; n += 2) store_pair(zb + ((size_t)m * ZKB + n / 2) * 1024, g[n], g[n + 1], fr, fq);
        *(u32x2*)(zs + (size_t)m * ZKB * 1024) = (u32x2){cvt_pk_bf16(g[6][0], g[6][1]), cvt_pk_bf16(g[6][2], g[6][3])};
      } else {
        *(u32x2*)(zs + (size_t)m * ZKB * 1024) = (u32x2){cvt_pk_bf16(g[0][0], g[0][1]), cvt_pk_bf16(g[0][2], g[0][3])};
#pragma unroll
        for (int n = 0; n < 6; n += 2) store_pair(zb + ((size_t)m * ZKB + n / 2) * 1024, g[n + 1], g[n + 2], fr, fq);
      }
    }
  }
}

__global__ __launch_bounds__(256, 1) void k_stage2(const u16* __restrict__ wd_re, const u16* __restrict__ wd_im, const u16* __restrict__ w2, unsigned char* __restrict__ ws) {
  extern __shared__ __attribute__((aligned(16))) unsigned char shm[];
  const int wid = __builtin_amdgcn_readfirstlane(threadIdx.x >> 6), lane = threadIdx.x & 63, wm = wid >> 1, wn = wid & 1;
  const int xcd = blockIdx.x & 7, slot = blockIdx.x >> 3, role = xcd >> 2;
  const int pm = (xcd & 1) * 8 + (slot & 7), pn = ((xcd >> 1) & 1) * 4 + (slot >> 3);
  Pipe<G2> p; p.lds = (unsigned)(size_t)(LAS unsigned char*)shm; p.wid = wid;
  f32x4 acc[G2::MT][G2::NT];
  float* P = (float*)(ws + WS_P);
  const size_t tile_off = ((size_t)(pm * 8 + pn) * 4 + wid) * 4096 + lane * 4;
  const int nseg = role ? 3 : 1;
  for (int seg = 0; seg < nseg; ++seg) {
    if (seg != 2) {
#pragma unroll
      for (int m = 0; m < G2::MT; ++m)
#pragma unroll
        for (int n = 0; n < G2::NT; ++n) acc[m][n] = (f32x4){0.f, 0.f, 0.f, 0.f};
    }
    if (seg == 0) {
      const int k0 = role ? 2816 : 0, nt = role ? 12 : 44;
      p.A.init_frag(ws + WS_Z, S_ * ZK * 2, ZKB, pm * 128, 2048 + k0, wid, lane);
      p.B.init_nmaj(w2, F_ * D_ * 2, D_, pn * 128, k0, wid, lane);
      gemm_loop<G2, FRAG, NMAJ>(p, lane, acc, nt);
    } else {
      p.A.init_frag(ws + WS_Z, S_ * ZK * 2, ZKB, pm * 128, (seg - 1) * 1024, wid, lane);
      p.B.init_rowk(seg == 1 ? wd_re : wd_im, D_ * D_ * 2, D_, pn * 128, 0, wid, lane);
      gemm_loop<G2, FRAG, ROWK>(p, lane, acc, 16);
    }
    bar();
    if (seg != 1) {
      float* dst = P + (size_t)(seg == 0 ? role : 2) * (S_ * D_) + tile_off;
#pragma unroll
      for (int m = 0; m < G2::MT; ++m)
#pragma unroll
        for (int n = 0; n < G2::NT; ++n) *(f32x4*)(dst + (m * G2::NT + n) * 256) = acc[m][n];
    }
  }
}

__global__ __launch_bounds__(256) void k_final(const u16* __restrict__ x, const u16* __restrict__ b2, const unsigned char* __restrict__ ws, u16* __restrict__ out) {
  const float* P = (const float*)(ws + WS_P);
  const int i = blockIdx.x * 256 + threadIdx.x;
  const int lane = i & 63, mn = (i >> 6) & 15, wid = (i >> 10) & 3, tile = i >> 12;
  const int row = (tile >> 3) * 128 + (wid >> 1) * 64 + (mn >> 2) * 16 + (lane & 15), col = (tile & 7) * 128 + (wid & 1) * 64 + (mn & 3) * 16 + (lane >> 4) * 4;
  const f32x4 o = *(const f32x4*)(P + (size_t)i * 4) + *(const f32x4*)(P + (size_t)S_ * D_ + (size_t)i * 4), y = *(const f32x4*)(P + (size_t)2 * S_ * D_ + (size_t)i * 4);
  const u32x2 xb = *(const u32x2*)(x + (size_t)row * D_ + col), bb = *(const u32x2*)(b2 + col);
  const float xv[4] = {bf_lo(xb[0]), bf_hi(xb[0]), bf_lo(xb[1]), bf_hi(xb[1])}, bv[4] = {bf_lo(bb[0]), bf_hi(bb[0]), bf_lo(bb[1]), bf_hi(bb[1])};
  float r[4];
#pragma unroll
  for (int j = 0; j < 4; ++j) r[j] = xv[j] + y[j] * silu(o[j] + bv[j]);
  *(u32x2*)(out + (size_t)row * D_ + col) = (u32x2){cvt_pk_bf16(r[0], r[1]), cvt_pk_bf16(r[2], r[3])};
}

extern "C" void kernel_launch(void* const* d_in, const int* in_sizes, int n_in, void* d_out, int out_size, void* d_ws, size_t ws_size, hipStream_t stream) {
  const u16* x = (const u16*)d_in[0]; const u16* ws_re = (const u16*)d_in[1]; const u16* ws_im = (const u16*)d_in[2];
  const u16* wd_re = (const u16*)d_in[3]; const u16* wd_im = (const u16*)d_in[4];
  const u16* w1 = (const u16*)d_in[6]; const u16* b1 = (const u16*)d_in[7]; const u16* w2 = (const u16*)d_in[8]; const u16* b2 = (const u16*)d_in[9];
  unsigned char* ws = (unsigned char*)d_ws;
  (void)hipFuncSetAttribute((const void*)k_stage1, hipFuncAttributeMaxDynamicSharedMemorySize, LDS1);
  (void)hipFuncSetAttribute((const void*)k_stage2, hipFuncAttributeMaxDynamicSharedMemorySize, LDS2);
  k_ln<<<S_ / 8, 128, 0, stream>>>(x, ws);
  k_stage1<<<256, 256, LDS1, stream>>>(ws_re, ws_im, w1, b1, ws);
  k_stage2<<<256, 256, LDS2, stream>>>(wd_re, wd_im, w2, ws);
  k_final<<<S_ * D_ / 4 / 256, 256, 0, stream>>>(x, b2, ws, (u16*)d_out);
}
```

```cpp
#include <hip/hip_runtime.h>
#include <hip/hip_bf16.h>
#include <type_traits>
#include <utility>

#define LAS __attribute__((address_space(3)))
#define DI __device__ __forceinline__
typedef unsigned short u16;
typedef short bf16x4 __attribute__((ext_vector_type(4)));
typedef float f32x4 __attribute__((ext_vector_type(4)));
typedef float f32x2 __attribute__((ext_vector_type(2)));
typedef unsigned u32x4 __attribute__((ext_vector_type(4)));
typedef unsigned u32x2 __attribute__((ext_vector_type(2)));

constexpr int S_ = 2048, D_ = 1024, F_ = 3584;
constexpr size_t WS_FLAG = 0;
constexpr int FL_AB = 0, FL_H = 16, FL_W2 = 24, FL_Y = 32, FL_N = 32 + 128;
constexpr size_t WS_XA = 65536;
constexpr size_t WS_AB = WS_XA + (size_t)S_ * D_ * 2;
constexpr size_t WS_H = WS_AB + (size_t)S_ * 2 * D_ * 2;
constexpr size_t WS_W2 = WS_H + (size_t)S_ * F_ * 2;
constexpr size_t WS_PY = WS_W2 + (size_t)F_ * D_ * 2;
constexpr unsigned OOB = 0x40000000u;
constexpr int SPIN_CAP = 1 << 20;
constexpr int W1P = 256;

template <int N> DI void wait_vm() { asm volatile("s_waitcnt vmcnt(%0)" ::"n"(N) : "memory"); }
DI void wait_lgkm0() { asm volatile("s_waitcnt lgkmcnt(0)" ::: "memory"); }
template <int N> DI void wait_lgkm() { asm volatile("s_waitcnt lgkmcnt(%0)" ::"n"(N > 15 ? 15 : N) : "memory"); }
DI void bar() { asm volatile("s_barrier" ::: "memory"); }
DI unsigned cvt_pk_bf16(float lo, float hi) {
  typedef __bf16 bf16x2 __attribute__((ext_vector_type(2)));
  f32x2 v = {lo, hi}; bf16x2 b = __builtin_convertvector(v, bf16x2); return __builtin_bit_cast(unsigned, b);
}
DI float bf_lo(unsigned w) { return __uint_as_float(w << 16); }
DI float bf_hi(unsigned w) { return __uint_as_float(w & 0xffff0000u); }
DI float rbf(float v) { return bf_lo(cvt_pk_bf16(v, v)); }
template <int I, int N, class F> DI void static_for(F&& f) { if constexpr (I < N) { f(std::integral_constant<int, I>{}); static_for<I + 1, N>(f); } }

#define ACLOB "a0","a1","a2","a3","a4","a5","a6","a7","a8","a9","a10","a11","a12","a13","a14","a15","a16","a17","a18","a19","a20","a21","a22","a23","a24","a25","a26","a27","a28","a29","a30","a31","a32","a33","a34","a35","a36","a37","a38","a39","a40","a41","a42","a43","a44","a45","a46","a47","a48","a49","a50","a51","a52","a53","a54","a55","a56","a57","a58","a59","a60","a61","a62","a63","a64","a65","a66","a67","a68","a69","a70","a71","a72","a73","a74","a75","a76","a77","a78","a79","a80","a81","a82","a83","a84","a85","a86","a87","a88","a89","a90","a91","a92","a93","a94","a95","a96","a97","a98","a99","a100","a101","a102","a103","a104","a105","a106","a107","a108","a109","a110","a111","a112","a113","a114","a115","a116","a117","a118","a119","a120","a121","a122","a123","a124","a125","a126","a127"
template <int RA, int RB> DI void mfma16(f32x4& acc) { asm volatile("v_mfma_f32_16x16x32_bf16 %0, a[%c1:%c2], a[%c3:%c4], %0" : "+v"(acc) : "n"(RA), "n"(RA + 3), "n"(RB), "n"(RB + 3)); }
template <int RD, int OFF> DI void ds_r128(unsigned addr) { asm volatile("ds_read_b128 a[%c1:%c2], %0 offset:%c3" ::"v"(addr), "n"(RD), "n"(RD + 3), "n"(OFF) : "memory"); }
template <int RD, int OFF> DI void ds_rtr(unsigned addr) { asm volatile("ds_read_b64_tr_b16 a[%c1:%c2], %0 offset:%c3" ::"v"(addr), "n"(RD), "n"(RD + 1), "n"(OFF) : "memory"); }
DI void wait_frags() { asm volatile("s_waitcnt lgkmcnt(0)" ::: "memory", ACLOB); }
template <int OFF, int POL> DI void dma16(int voff, __amdgpu_buffer_rsrc_t r, unsigned soff) {
  if constexpr (POL == 0) asm volatile("buffer_load_dwordx4 %0, %1, %2 offen offset:%3 lds" ::"v"(voff), "s"(r), "s"(soff), "n"(OFF) : "memory");
  if constexpr (POL == 1) asm volatile("buffer_load_dwordx4 %0, %1, %2 offen offset:%3 sc1 lds" ::"v"(voff), "s"(r), "s"(soff), "n"(OFF) : "memory");
  if constexpr (POL == 2) asm volatile("buffer_load_dwordx4 %0, %1, %2 offen offset:%3 nt lds" ::"v"(voff), "s"(r), "s"(soff), "n"(OFF) : "memory");
}
DI void pinacc(f32x4& v) { asm volatile("" : "+v"(v)); }
template <int C> DI int s_eq(int v) { int r; asm volatile("s_cmp_eq_u32 %1, %2\n\ts_cselect_b32 %0, 1, 0" : "=s"(r) : "s"(v), "n"(C) : "scc"); return r; }
DI __amdgpu_buffer_rsrc_t mkrsrc(const void* p, unsigned bytes) { return __builtin_amdgcn_make_buffer_rsrc((void*)p, 0, bytes, 0x00020000); }

#define STAMP(k, i) do {} while (0)
#define STAGGER 0
#define SNAKE 1
enum { ROWK = 0, FRAG = 1, NMAJ = 2 };

template <int KIND, int R, int POL> struct Stream {
  static constexpr bool ODD = KIND == NMAJ && R == 224;
  static constexpr int P = R / 32, NG = (P + 3) / 4, NV = ODD ? 7 : (KIND == FRAG ? 2 : 4);
  __amdgpu_buffer_rsrc_t rs;
  int voff[NV];
  unsigned s0[NG], ks;
  unsigned ml[NG];
  static DI int gpiece(int g, int wid) { if constexpr (ODD) return wid * 7 + 4 * g; else return 16 * g + 4 * wid; }
  DI void init_rowk(const void* p, unsigned bytes, int ld, int r0, int k0, int wid, int lane) {
    rs = mkrsrc(p, bytes);
#pragma unroll
    for (int j = 0; j < 4; ++j) voff[j] = ((lane >> 3) + 8 * j) * ld * 2 + (((lane & 7) ^ ((4 * (j & 1) + (lane >> 4)) & 7)) * 16) - j * 1024;
#pragma unroll
    for (int g = 0; g < NG; ++g) s0[g] = (unsigned)((r0 + 8 * gpiece(g, wid)) * ld + k0) * 2u;
    ks = 128u;
  }
  DI void init_frag(const void* p, unsigned bytes, int kbn, int r0, int k0, int wid, int lane) {
    rs = mkrsrc(p, bytes);
    voff[0] = lane * 16; voff[1] = lane * 16 + kbn * 1024 - 2048;
#pragma unroll
    for (int g = 0; g < NG; ++g) s0[g] = (unsigned)((r0 / 16 + gpiece(g, wid) / 2) * kbn + k0 / 32) * 1024u;
    ks = 2048u;
  }
  DI void init_nmaj(const void* p, unsigned bytes, int ld, int n0, int k0, int wid, int lane) {
    rs = mkrsrc(p, bytes);
    if constexpr (ODD) {
#pragma unroll
      for (int j = 0; j < 7; ++j) { const int L = (wid * 7 + j) * 64 + lane, kk = L / 28, pos = L % 28;
        voff[j] = kk * ld * 2 + (pos ^ (((kk >> 3) & 1) << 1)) * 16 - (j & 3) * 1024; }
      s0[0] = s0[1] = (unsigned)(k0 * ld + n0) * 2u;
    } else {
      static_assert(R == 128 || ODD, "");
#pragma unroll
      for (int j = 0; j < 4; ++j) { const int kk = 4 * (4 * wid + j) + (lane >> 4), g = (kk & 3) | (((kk >> 3) & 1) << 2);
        voff[j] = (4 * j + (lane >> 4)) * ld * 2 + (((lane & 15) ^ (g << 1)) * 16) - j * 1024; }
      s0[0] = (unsigned)((k0 + 16 * wid) * ld + n0) * 2u;
    }
    ks = (unsigned)(64 * ld) * 2u;
  }
  DI void init_lds(unsigned base, int wid) {
#pragma unroll
    for (int g = 0; g < NG; ++g) ml[g] = base + (unsigned)gpiece(g, wid) * 1024u;
  }
  template <int I, int SO> DI void m0() const { asm volatile("s_add_u32 m0, %0, %1" ::"s"(ml[I / 4]), "n"(SO) : "memory", "scc"); }
  template <int I> DI void issue() const {
    constexpr int g = I / 4, j = I % 4, vi = ODD ? I : (KIND == FRAG ? (j >> 1) : j);
    dma16<j * 1024, POL>(voff[vi], rs, s0[g]);
  }
  template <int I> DI void advance() { asm volatile("s_add_u32 %0, %0, %1" : "+s"(s0[I / 4]) : "s"(ks) : "scc"); }
};

template <int KIND, int R, int T, int NS, int STAGE> struct Reader {
  static constexpr int NA = KIND == FRAG ? 1 : (KIND == ROWK ? 2 : 3);
  static constexpr int SPAN = KIND == NMAJ ? 64 * R * 2 : T * 2048, SPW = (65536 - SPAN) / STAGE + 1, NW = (NS + SPW - 1) / SPW;
  unsigned a[NW][NA];
  DI void init(unsigned base, int t0, int lane) {
    const int fr = lane & 15, fq = lane >> 4;
    if constexpr (KIND == ROWK) { a[0][0] = base + (t0 * 16 + fr) * 128 + ((fq ^ (fr >> 1)) * 16); a[0][1] = base + (t0 * 16 + fr) * 128 + (((4 + fq) ^ (fr >> 1)) * 16); }
    else if constexpr (KIND == FRAG) a[0][0] = base + t0 * 2048 + lane * 16;
    else { static_assert(KIND != NMAJ || R == 224, "");
      const int q = fr >> 2, p = lane & 3, wn = t0 / 7; const unsigned b = base + (8 * fq + q) * 448 + 8 * p;
      a[0][0] = b + 32 * ((6 * wn) ^ (fq & 1)); a[0][1] = b + 32 * ((6 * wn + 1) ^ (fq & 1)); a[0][2] = b + 32 * ((12 + wn) ^ (fq & 1)); }
#pragma unroll
    for (int w = 1; w < NW; ++w)
#pragma unroll
      for (int n = 0; n < NA; ++n) { a[w][n] = a[0][n] + w * SPW * STAGE; asm volatile("" : "+v"(a[w][n])); }
  }
  template <int ST, int KS, int I, int RB> DI void rd() const {
    constexpr int w = ST / SPW, so = (ST % SPW) * STAGE;
    if constexpr (KIND == ROWK) ds_r128<RB + 4 * I, so + I * 2048>(a[w][KS]);
    else if constexpr (KIND == FRAG) ds_r128<RB + 4 * I, so + I * 2048 + KS * 1024>(a[w][0]);
    else { constexpr int n = I / 2, h = I % 2; ds_rtr<RB + 4 * n + 2 * h, so + (32 * KS + 4 * h) * (R * 2) + (n < 6 ? 32 * (n & ~1) : 0)>(a[w][n < 6 ? (n & 1) : 2]); }
  }
};

template <int BM_, int BN_, int NS_, int KA_, int KB_, int PLA_, int PLB_, bool TR_, int NX_, bool LAZY_, int RSP_> struct Geo {
  static constexpr bool LAZY = LAZY_; static constexpr int NX = NX_, PLA = PLA_, PLB = PLB_;
  static constexpr int BM = BM_, BN = BN_, NS = NS_, KA = KA_, KB = KB_, MT = BM / 32, NT = BN / 32, NM = MT * NT;
  static constexpr bool TR = TR_;
  static constexpr int PA = BM / 32, PB = BN / 32, ND = PA + PB, STAGE = (BM + BN) * 128;
  static constexpr int DEPTH = LAZY ? NS - 1 : NS;
  static constexpr int H = (DEPTH == 2) ? ND : ND / 2;
  static constexpr int NIA = KA == NMAJ ? 2 * MT : MT, NIB = KB == NMAJ ? 2 * NT : NT, NR = NIA + NIB, RSPAN = RSP_;
  static constexpr int rgap(int r) { return (r * RSPAN) / NR; }
  static constexpr int rbefore(int i) { int c = 0; for (int r = 0; r < NR; ++r) c += rgap(r) < i; return c; }
  typedef Reader<KA, BM, MT, NS, STAGE> RA; typedef Reader<KB, BN, NT, NS, STAGE> RB;
};
struct Side { __amdgpu_buffer_rsrc_t rs; unsigned voff, step, soff; };
template <class G> struct Pipe {
  Stream<G::KA, G::BM, G::PLA> A; Stream<G::KB, G::BN, G::PLB> B;
  Side xl, x2;
  unsigned ls = 0;
  unsigned lds; int wid;
  DI void side() {
    if constexpr (G::NX > 0) asm volatile("buffer_load_dword a124, %0, %1, %2 offen" ::"v"(xl.voff), "s"(xl.rs), "s"(xl.soff) : "memory");
    if constexpr (G::NX > 1) asm volatile("buffer_load_dword a124, %0, %1, %2 offen" ::"v"(x2.voff), "s"(x2.rs), "s"(x2.soff) : "memory"); }
  DI void side_adv() {
    if constexpr (G::NX > 0) asm volatile("s_add_u32 %0, %0, %1" : "+s"(xl.soff) : "s"(xl.step) : "scc");
    if constexpr (G::NX > 1) asm volatile("s_add_u32 %0, %0, %1" : "+s"(x2.soff) : "s"(x2.step) : "scc"); }
  static constexpr bool gstart(int i) { return i < G::PA ? (i % 4 == 0) : ((i - G::PA) % 4 == 0); }
  static constexpr bool gend(int i) { return i == G::PA - 1 || i == G::ND - 1 || gstart(i + 1); }
  template <int I, int ST> DI void m0() const { if constexpr (I < G::PA) A.template m0<I, ST * G::STAGE>(); else B.template m0<I - G::PA, ST * G::STAGE>(); }
  template <int I> DI void issue() const { if constexpr (I < G::PA) A.template issue<I>(); else B.template issue<I - G::PA>(); }
  template <int I> DI void advance() { if constexpr (I < G::PA) A.template advance<I>(); else B.template advance<I - G::PA>(); }
  template <int MASK> DI void prologue() {
    if constexpr (MASK & 1) A.init_lds(lds, wid);
    if constexpr (MASK & 2) B.init_lds(lds + G::BM * 128, wid);
    asm volatile("s_nop 4" ::: "memory", ACLOB);
    static_for<0, G::DEPTH>([&](auto Ss) {
      if constexpr (MASK == 3 && Ss.value >= 2) { side(); side_adv(); }
      static_for<0, (Ss.value == G::DEPTH - 1 ? G::H : G::ND)>([&](auto Dd) { constexpr int d = Dd.value;
        if constexpr ((MASK >> (d < G::PA ? 0 : 1)) & 1) {
          if constexpr (gstart(d)) { m0<d, Ss.value>(); asm volatile("s_nop 0"); }
          issue<d>();
          if constexpr (gend(d)) advance<d>(); } }); });
  }
};

constexpr int dgap(int d, int ndp, int nm, int w) { return 1 + (d * (nm - 2) + w * (nm - 2) / 4) / ndp; }
constexpr int dat(int i, int ndp, int nm, int w) { for (int d = 0; d < ndp; ++d) if (dgap(d, ndp, nm, w) == i) return d; return -1; }
template <class G, int W, int CUR, int RST, int RKS, int D0, int D1, int DST, int I>
DI void gap(Pipe<G>& p, const typename G::RA& ra, const typename G::RB& rb, f32x4 (&acc)[G::MT][G::NT]) {
  constexpr int CA = 64 * CUR, CB = CA + 32, NA = 64 * (CUR ^ 1), NB = NA + 32;
  constexpr int NM = G::NM, NIB = G::NIB, NR = G::NR, NDP = D1 - D0, m = I / G::NT, n0 = I % G::NT, n = (SNAKE && (m & 1)) ? G::NT - 1 - n0 : n0;
  constexpr int d = dat(I, NDP, NM, W), dp = dat(I - 1, NDP, NM, W);
  if constexpr (n0 == 0 && m % 2 == 0 && (G::LAZY || CUR == 0)) wait_lgkm<(NR - 1 - (NIB + m + 1)) + G::rbefore(I)>();
  if constexpr (d >= 0) if constexpr (Pipe<G>::gstart(D0 + d)) p.template m0<D0 + d, DST>();
  if constexpr (G::TR) mfma16<CB + 4 * n, CA + 4 * m>(acc[m][n]); else mfma16<CA + 4 * m, CB + 4 * n>(acc[m][n]);
  if constexpr (I == 0 && CUR == (G::DEPTH == 2 ? 0 : 1)) p.side();
  if constexpr (I == NM - 1 && CUR == (G::DEPTH == 2 ? 0 : 1)) p.side_adv();
  if constexpr (dp >= 0) if constexpr (Pipe<G>::gend(D0 + dp)) p.template advance<D0 + dp>();
  if constexpr (d >= 0) p.template issue<D0 + d>();
  static_for<G::rbefore(I), G::rbefore(I + 1)>([&](auto Rr) { constexpr int r = Rr.value;
    if constexpr (r == 0) ra.template rd<RST, RKS, 0, NA>();
    else if constexpr (r <= NIB) rb.template rd<RST, RKS, r - 1, NB>();
    else ra.template rd<RST, RKS, r - NIB, NA>(); });
}
template <class G, int W, int CUR, int RST, int RKS, int D0, int D1, int DST, int... Is>
DI void phase_(Pipe<G>& p, const typename G::RA& ra, const typename G::RB& rb, f32x4 (&acc)[G::MT][G::NT], std::integer_sequence<int, Is...>) {
  (gap<G, W, CUR, RST, RKS, D0, D1, DST, Is>(p, ra, rb, acc), ...);
}
template <class G, int W, int ST>
DI void ktile(Pipe<G>& p, const typename G::RA& ra, const typename G::RB& rb, f32x4 (&acc)[G::MT][G::NT]) {
  constexpr int NS = G::NS, ND = G::ND, H = G::H, DP = G::DEPTH;
  phase_<G, W, 0, ST, 1, H, ND, (ST + DP - 1) % NS>(p, ra, rb, acc, std::make_integer_sequence<int, G::NM>{});
  if constexpr (G::LAZY) asm volatile("s_waitcnt vmcnt(%0)\n\ts_barrier" ::"n"((DP - 2) * (ND + G::NX)) : "memory", ACLOB);
  else asm volatile("s_waitcnt vmcnt(%0) lgkmcnt(0)\n\ts_barrier" ::"n"(DP == 2 ? G::NX : (DP - 2) * (ND + G::NX)) : "memory", ACLOB);
  phase_<G, W, 1, (ST + 1) % NS, 0, 0, H, (ST + DP) % NS>(p, ra, rb, acc, std::make_integer_sequence<int, G::NM>{});
}
template <class G, int W, int NTILE>
DI void gemm_body(Pipe<G>& p, const typename G::RA& ra, const typename G::RB& rb, f32x4 (&acc)[G::MT][G::NT], int on) {
#pragma nounroll
  for (int it = on * (NTILE / G::NS); it > 0; --it) { static_for<0, G::NS>([&](auto Ss) { ktile<G, W, Ss.value>(p, ra, rb, acc); });
  }
  if constexpr (NTILE % G::NS != 0) {
    asm volatile("" : "+s"(on));
#pragma nounroll
    for (int it = on; it > 0; --it) static_for<0, NTILE % G::NS>([&](auto Ss) { ktile<G, W, Ss.value>(p, ra, rb, acc); });
  }
}
template <class G, int NTILE, bool SPLIT, class Init>
DI void gemm_loop(Pipe<G>& p, int lane, f32x4 (&acc)[G::MT][G::NT], int st0, Init init) {
  constexpr int ND = G::ND, H = G::H, DP = G::DEPTH;
  typename G::RA ra; typename G::RB rb;
  ra.init(p.lds, (p.wid >> 1) * G::MT, lane); rb.init(p.lds + G::BM * 128, (p.wid & 1) * G::NT, lane);
#pragma unroll
  for (int n = 0; n < G::NT; ++n) { const f32x4 v = init(n);
#pragma unroll
    for (int m = 0; m < G::MT; ++m) { acc[m][n] = v; pinacc(acc[m][n]); } }
  if constexpr (SPLIT && H < ND) if constexpr (!Pipe<G>::gstart(H)) p.template m0<H, DP - 1>();
  wait_vm<(SPLIT ? 0 : (DP - 2) * (ND + G::NX) + H)>();
  bar();
  static_for<0, G::NIA>([&](auto I) { ra.template rd<0, 0, I.value, 0>(); });
  static_for<0, G::NIB>([&](auto I) { rb.template rd<0, 0, I.value, 32>(); });
  wait_frags();
  STAMP(1, st0);
  asm volatile("s_nop 1");
  gemm_body<G, 0, NTILE>(p, ra, rb, acc, 1);
  asm volatile("s_nop 7\n\ts_nop 7\n\ts_waitcnt vmcnt(0) lgkmcnt(0)" ::: "memory", ACLOB);
#pragma unroll
  for (int m = 0; m < G::MT; ++m)
#pragma unroll
    for (int n = 0; n < G::NT; ++n) pinacc(acc[m][n]);
  STAMP(1, st0 + 1);
}

DI void touch(unsigned& sink, const void* p) { asm volatile("global_load_dword %0, %1, off" : "+v"(sink) : "v"(p)); }
DI float wave_sum(float a) {
#pragma unroll
  for (int o = 1; o < 64; o <<= 1) a += __shfl_xor(a, o);
  return a;
}
__global__ __launch_bounds__(512) void k_ln(const u16* __restrict__ x, const u16* __restrict__ wd_re, const u16* __restrict__ wd_im, const u16* __restrict__ w1,
                                            const u16* __restrict__ b1, const u16* __restrict__ b2, unsigned char* __restrict__ ws) {
  __shared__ __attribute__((aligned(16))) unsigned char tl[128 * 144];
  const int tid = threadIdx.x, lane = tid & 63, w = tid >> 6, r0 = blockIdx.x * 8;
  STAMP(0, 0);
  u32x4 raw[2];
  { const u16* px = x + (size_t)(r0 + w) * D_ + lane * 8;
    asm volatile("global_load_dwordx4 %0, %2, off nt\n\tglobal_load_dwordx4 %1, %2, off offset:1024 nt" : "=&v"(raw[0]), "=&v"(raw[1]) : "v"(px)); }
  unsigned sink = 0;
  { const int g = tid * 256 + blockIdx.x;
    const int l = g - W1P * 56;
    const char* p = l < 0 ? (const char*)w1 + (size_t)g * 128 : (l < 32768 ? (const char*)(l < 16384 ? wd_re : wd_im) + (size_t)(l & 16383) * 128 : ((l & 127) < 56 ? (const char*)b1 + (l & 127) * 128 : (const char*)b2 + (l & 15) * 128));
    asm volatile("global_load_dword %0, %1, off" : "+v"(sink) : "v"(p)); }
  asm volatile("s_waitcnt vmcnt(1)" : "+v"(raw[0]), "+v"(raw[1]));
  float v[16];
#pragma unroll
  for (int j = 0; j < 2; ++j)
#pragma unroll
    for (int e = 0; e < 4; ++e) { v[8 * j + 2 * e] = bf_lo(raw[j][e]); v[8 * j + 2 * e + 1] = bf_hi(raw[j][e]); }
  float a = 0.f;
#pragma unroll
  for (int e = 0; e < 16; ++e) a += v[e];
  const float mean = wave_sum(a) * (1.0f / D_);
  a = 0.f;
#pragma unroll
  for (int e = 0; e < 16; ++e) { const float dd = v[e] - mean; a += dd * dd; }
  const float rden = 1.0f / rbf(sqrtf(rbf(rbf(wave_sum(a) * (1.0f / D_)) + 1e-5f))), mb = rbf(mean);
#pragma unroll
  for (int j = 0; j < 2; ++j) {
    u32x4 o;
#pragma unroll
    for (int e = 0; e < 4; ++e) o[e] = cvt_pk_bf16(rbf(v[8 * j + 2 * e] - mb) * rden, rbf(v[8 * j + 2 * e + 1] - mb) * rden);
    *(u32x4*)(tl + (j * 64 + lane) * 144 + w * 16) = o;
  }
  wait_lgkm0(); bar();
  unsigned char* xa = ws + WS_XA + (size_t)(r0 >> 4) * 32768 + (r0 & 8) * 16;
#pragma unroll
  for (int j = 0; j < 2; ++j) { const int ch = j * 64 + (tid >> 3);
    *(u32x4*)(xa + ch * 256 + (tid & 7) * 16) = *(const u32x4*)(tl + ch * 144 + (tid & 7) * 16); }
  if (tid == 0 && blockIdx.x < FL_N) *(unsigned*)(ws + WS_FLAG + (size_t)blockIdx.x * 128) = 0u;
  asm volatile("s_waitcnt vmcnt(0)" : "+v"(sink));
  STAMP(0, 3);
}

DI f32x2 sigm_mul(f32x2 x, f32x2 arg) {
  f32x2 e; e.x = __builtin_amdgcn_exp2f(arg.x); e.y = __builtin_amdgcn_exp2f(arg.y);
  const f32x2 d = e + 1.0f;
  f32x2 r; r.x = __builtin_amdgcn_rcpf(d.x); r.y = __builtin_amdgcn_rcpf(d.y);
  return x * r;
}
DI f32x2 gelu2(f32x2 x) {
  const float k0 = -2.0f * 0.7978845608028654f * 1.4426950408889634f, k1 = k0 * 0.044715f;
  return sigm_mul(x, x * ((x * x) * k1 + k0));
}
DI f32x2 silu2(f32x2 x) { return sigm_mul(x, x * -1.4426950408889634f); }
DI f32x4 gelu4(f32x4 t) { const f32x2 a = gelu2((f32x2){t[0], t[1]}), b = gelu2((f32x2){t[2], t[3]}); return (f32x4){a.x, a.y, b.x, b.y}; }
DI f32x4 bf4(u32x2 w) { return (f32x4){bf_lo(w[0]), bf_hi(w[0]), bf_lo(w[1]), bf_hi(w[1])}; }

#define GO_NS 4
#define GO_LAZY false
#define GO_RSP 11
#define DEVPOL 1
typedef Geo<256, 128, 3, FRAG, ROWK, 0, 0, false, 1, false, 24> GAB;
typedef Geo<256, 128, 3, ROWK, FRAG, 0, DEVPOL, true, 0, false, 24> GY;
#define H_TOUCH 1
typedef Geo<256, 224, 2, FRAG, NMAJ, 0, 0, true, H_TOUCH, false, 42> GH;
typedef Geo<128, 128, GO_NS, FRAG, FRAG, DEVPOL, DEVPOL, true, 0, GO_LAZY, GO_RSP> GO;
constexpr int LDSM = GY::NS * GY::STAGE > GO::NS * GO::STAGE ? GY::NS * GY::STAGE : GO::NS * GO::STAGE;

DI u32x4 pack_pair(const f32x4& a, const f32x4& b) {
  const unsigned a0 = cvt_pk_bf16(a[0], a[1]), a1 = cvt_pk_bf16(a[2], a[3]), b0 = cvt_pk_bf16(b[0], b[1]), b1 = cvt_pk_bf16(b[2], b[3]);
  const auto r0 = __builtin_amdgcn_permlane16_swap(a0, b0, false, false);
  const auto r1 = __builtin_amdgcn_permlane16_swap(a1, b1, false, false);
  return (u32x4){r0[0], r1[0], r0[1], r1[1]};
}
DI u32x4 pack8(const f32x4& a, const f32x4& b) { return (u32x4){cvt_pk_bf16(a[0], a[1]), cvt_pk_bf16(a[2], a[3]), cvt_pk_bf16(b[0], b[1]), cvt_pk_bf16(b[2], b[3])}; }
struct ZeroInit { DI f32x4 operator()(int) const { return (f32x4){0.f, 0.f, 0.f, 0.f}; } };
DI void wait_bias(u32x2& b) { asm volatile("s_waitcnt vmcnt(%1)" : "+v"(b) : "n"(2 * GH::ND)); }

DI void st_dev(__amdgpu_buffer_rsrc_t r, unsigned off, u32x4 v) { __builtin_amdgcn_raw_buffer_store_b128(v, r, off, 0, 16); }
DI void st_dev8(__amdgpu_buffer_rsrc_t r, unsigned off, u32x2 v) { __builtin_amdgcn_raw_buffer_store_b64(v, r, off, 0, 16); }
DI u32x4 ld_dev(__amdgpu_buffer_rsrc_t r, unsigned voff, unsigned soff) { return __builtin_amdgcn_raw_buffer_load_b128(r, voff, soff, 16); }
DI void publish(unsigned* cnt) {
  wait_vm<0>();
  bar();
  if (threadIdx.x == 0) __hip_atomic_fetch_add(cnt, 1u, __ATOMIC_RELAXED, __HIP_MEMORY_SCOPE_AGENT);
}
DI void poll(unsigned* f, unsigned want) {
#pragma nounroll
  for (int i = 0; i < SPIN_CAP && __hip_atomic_load(f, __ATOMIC_RELAXED, __HIP_MEMORY_SCOPE_AGENT) != want; ++i) __builtin_amdgcn_s_sleep(1); }
DI void await1(unsigned* f0, unsigned w0) {
  if (threadIdx.x == 0) poll(f0, w0);
  asm volatile("" ::: "memory");
  bar();
}
DI void await2(unsigned* f0, unsigned w0, unsigned* f1, unsigned w1) {
  if (threadIdx.x == 0) { poll(f0, w0); poll(f1, w1); }
  asm volatile("" ::: "memory");
  bar();
}

__global__ __launch_bounds__(256, 1) void k_main(const u16* __restrict__ x, const u16* __restrict__ ws_re, const u16* __restrict__ ws_im, const u16* __restrict__ wd_re, const u16* __restrict__ wd_im,
                                                 const u16* __restrict__ w1, const u16* __restrict__ b1, const u16* __restrict__ w2, const u16* __restrict__ b2, unsigned char* ws, u16* __restrict__ out) {
  extern __shared__ __attribute__((aligned(16))) unsigned char shm[];
  const char* pc; asm volatile("s_getpc_b64 %0" : "=s"(pc));
  const int wid = __builtin_amdgcn_readfirstlane(threadIdx.x >> 6), lane = threadIdx.x & 63, fr = lane & 15, fq = lane >> 4, wm = wid >> 1, wn = wid & 1;
  const int xcd = blockIdx.x & 7, slot = blockIdx.x >> 3, chain = slot >> 4, s4 = slot & 15;
  const int pm1 = (xcd & 1) * 4 + (s4 & 3), pn1 = (xcd >> 1) * 4 + (s4 >> 2);
  const int half = xcd & 1, pm = (xcd >> 2) * 4 + (s4 & 3), pn = ((xcd >> 1) & 1) * 4 + (s4 >> 2), tile = pm * 8 + pn;
  const unsigned ldsb = (unsigned)(size_t)(LAS unsigned char*)shm;
  const int lo = ((fq & 1) * 2 + (fq >> 1)) * 256 + fr * 16;
  unsigned* flags = (unsigned*)(ws + WS_FLAG);
  const __amdgpu_buffer_rsrc_t ry = mkrsrc(ws + WS_PY + (size_t)tile * 131072, 131072);
  STAMP(1, 0);
  if (chain == 0) {
    const int kb = xcd * 16 + s4, yt = kb * 256 + threadIdx.x;
    const u16* wsm = half ? ws_im : ws_re;
    const char* wsp = (const char*)wsm + (size_t)(pm * 256 + pn * 32 + (threadIdx.x >> 4)) * (S_ * 2) + (threadIdx.x & 15) * 128;
    unsigned sink = 0;
    {
      if (s4 == 0) touch(sink, pc + threadIdx.x * 128);
      touch(sink, (const char*)(w1 + (size_t)W1P * F_) + (size_t)yt * 128);
      if (yt + 32768 < (D_ - W1P) * 56) touch(sink, (const char*)(w1 + (size_t)W1P * F_) + (size_t)(yt + 32768) * 128);
      if (kb < 112) {
        const __amdgpu_buffer_rsrc_t rs = mkrsrc(w2 + (size_t)kb * 32 * D_, 32 * D_ * 2);
        asm volatile("s_nop 4" ::: "memory");
        static_for<0, 16>([&](auto I) { constexpr int i = I.value;
          const int g = ((i >> 1) & 3) | ((wid & 1) << 2);
          asm volatile("s_add_u32 m0, %0, %1\n\ts_nop 0" ::"s"(ldsb + wid * 16384), "n"(i * 1024) : "memory", "scc");
          dma16<0, 2>((lane ^ (g << 1)) * 16, rs, wid * 16384 + i * 1024); });
      }
      if (kb < 112) {
        wait_vm<(H_TOUCH ? 0 : 2)>(); bar();
        const int q = fr >> 2, gk = q | ((fq & 1) << 2);
        const __amdgpu_buffer_rsrc_t rw = mkrsrc(ws + WS_W2, F_ * D_ * 2);
        LAS unsigned char* src = (LAS unsigned char*)shm + (8 * fq + q) * 2048 + wid * 512 + 8 * (lane & 3);
#pragma unroll
        for (int n = 0; n < 16; ++n) {
          const bf16x4 l4 = __builtin_amdgcn_ds_read_tr16_b64_v4i16((LAS bf16x4*)(src + 32 * (n ^ gk)));
          const bf16x4 h4 = __builtin_amdgcn_ds_read_tr16_b64_v4i16((LAS bf16x4*)(src + 32 * (n ^ gk) + 8192));
          const u32x2 a = __builtin_bit_cast(u32x2, l4), b = __builtin_bit_cast(u32x2, h4);
          st_dev(rw, (unsigned)((wid * 16 + n) * 112 + kb) * 1024u + lane * 16, (u32x4){a[0], a[1], b[0], b[1]});
        }
        wait_lgkm0();
        publish(flags + FL_W2 * 32);
      }
      STAMP(1, 4);
    }
    f32x4 acc[8][4];
    {
      Pipe<GAB> p; p.lds = ldsb; p.wid = wid;
      p.xl.rs = mkrsrc(wsm + (size_t)(pm * 256 + pn * 32 + wm * 16) * S_, 16 * S_ * 2); p.xl.step = S_ * 2; p.xl.soff = 2048; p.xl.voff = lane < 8 ? (unsigned)(wn * 1024 + lane * 128) : OOB;
      p.A.init_frag(ws + WS_XA, S_ * D_ * 2, 32, pm1 * 256, 0, wid, lane);
      p.B.init_rowk(pn1 < 8 ? wd_re : wd_im, D_ * D_ * 2, D_, (pn1 & 7) * 128, 0, wid, lane);
      p.prologue<3>();
      gemm_loop<GAB, 16, false>(p, lane, acc, 1, ZeroInit());
      const __amdgpu_buffer_rsrc_t ro = mkrsrc(ws + WS_AB, S_ * 2 * D_ * 2);
      const unsigned ob = (unsigned)((pn1 * 8 + wn * 4) * 64 + pm1 * 8 + wm * 4) * 1024u + lo;
#pragma unroll
      for (int n = 0; n < 4; ++n)
#pragma unroll
        for (int m = 0; m < 8; m += 2) st_dev(ro, ob + (n * 64 + m / 2) * 1024, pack_pair(acc[m][n], acc[m + 1][n]));
      publish(flags + (FL_AB + pn1) * 32);
      STAMP(1, 3);
    }
    {
      Pipe<GY> p; p.lds = ldsb; p.wid = wid;
      p.A.init_rowk(wsm, S_ * S_ * 2, S_, pm * 256, 0, wid, lane);
      p.B.init_frag(ws + WS_AB, S_ * 2 * D_ * 2, 64, half * D_ + pn * 128, 0, wid, lane);
      p.prologue<1>();
      await1(flags + (FL_AB + half * 8 + pn) * 32, 8);
      STAMP(1, 5);
      p.prologue<2>();
      gemm_loop<GY, 32, true>(p, lane, acc, 6, ZeroInit());
      asm volatile("" : "+v"(sink));
#pragma unroll
      for (int i = 0; i < 16; ++i) st_dev(ry, lane * 16 + half * 65536 + wm * 32768 + (((i >> 3) * 2 + wn) * 8 + (i & 7)) * 1024, pack8(acc[i >> 1][(i & 1) * 2], acc[i >> 1][(i & 1) * 2 + 1]));
      publish(flags + (FL_Y + tile * 2 + half) * 32);
      STAMP(1, 8);
    }
  } else {
    {
      const int c0 = pn1 * 224;
      Pipe<GH> p; p.lds = ldsb; p.wid = wid;
      p.xl.rs = mkrsrc((half ? ws_im : ws_re) + (size_t)(pm * 256 + pn * 32 + wm * 16) * S_, 16 * S_ * 2); p.xl.step = S_ * 2; p.xl.soff = 0; p.xl.voff = lane < 8 ? (unsigned)(wn * 1024 + lane * 128) : OOB;
      u32x2 bias[7];
#pragma unroll
      for (int n = 0; n < 7; ++n) asm volatile("global_load_dwordx2 %0, %1, off" : "=v"(bias[n]) : "v"(b1 + c0 + (n < 6 ? wn * 96 + n * 16 : 192 + wn * 16) + fq * 4));
      p.A.init_frag(ws + WS_XA, S_ * D_ * 2, 32, pm1 * 256, 0, wid, lane);
      p.B.init_nmaj(w1, D_ * F_ * 2, F_, c0, 0, wid, lane);
      p.prologue<3>();
      f32x4 acc[8][7];
      gemm_loop<GH, 16, false>(p, lane, acc, 1, [&](int n) { wait_bias(bias[n]); return bf4(bias[n]); });
      const __amdgpu_buffer_rsrc_t ro = mkrsrc(ws + WS_H, S_ * F_ * 2);
      const unsigned ob = (unsigned)((pm1 * 16 + wm * 8) * 112 + pn1 * 7) * 1024u, op = ob + wn * 3072 + lo, os = ob + 6144 + (wn * 2 + (fq >> 1)) * 256 + fr * 16 + (fq & 1) * 8;
#pragma unroll
      for (int m = 0; m < 8; ++m) {
#pragma unroll
        for (int n = 0; n < 6; n += 2) st_dev(ro, op + (m * 112 + n / 2) * 1024, pack_pair(gelu4(acc[m][n]), gelu4(acc[m][n + 1])));
        const f32x4 g = gelu4(acc[m][6]); st_dev8(ro, os + m * 112 * 1024, (u32x2){cvt_pk_bf16(g[0], g[1]), cvt_pk_bf16(g[2], g[3])});
      }
      STAMP(1, 3);
      publish(flags + (FL_H + pm1) * 32);
      STAMP(1, 4);
    }
    f32x4 acc[4][4];
    Pipe<GO> p; p.lds = ldsb; p.wid = wid;
    p.A.init_frag(ws + WS_H, S_ * F_ * 2, 112, pm * 256 + half * 128, 0, wid, lane);
    p.B.init_frag(ws + WS_W2, F_ * D_ * 2, 112, pn * 128, 0, wid, lane);
    u32x2 bias[4]; u32x4 xr[8];
    { const size_t xo = (size_t)(pm * 256 + half * 128 + wm * 64 + (lane >> 3)) * D_ + pn * 128 + wn * 64 + (lane & 7) * 8;
#pragma unroll
      for (int n = 0; n < 4; ++n) asm volatile("global_load_dwordx2 %0, %1, off" : "=v"(bias[n]) : "v"(b2 + pn * 128 + wn * 64 + n * 16 + fq * 4));
#pragma unroll
      for (int j = 0; j < 8; ++j) asm volatile("global_load_dwordx4 %0, %1, off nt" : "=v"(xr[j]) : "v"(x + xo + (size_t)j * 8 * D_)); }
    await1(flags + FL_W2 * 32, 112);
    p.prologue<2>();
    await1(flags + (FL_H + pm) * 32, 16);
    STAMP(1, 5);
    p.prologue<1>();
    const size_t xo = (size_t)(pm * 256 + half * 128 + wm * 64 + (lane >> 3)) * D_ + pn * 128 + wn * 64 + (lane & 7) * 8;
    gemm_loop<GO, 56, true>(p, lane, acc, 6, ZeroInit());
    await2(flags + (FL_Y + tile * 2) * 32, 1, flags + (FL_Y + tile * 2 + 1) * 32, 1);
    STAMP(1, 8);
    u32x4 yr[8], yi[8];
#pragma unroll
    for (int j = 0; j < 8; ++j) asm volatile("" : "+v"(xr[j]));
#pragma unroll
    for (int n = 0; n < 4; ++n) asm volatile("" : "+v"(bias[n]));
#pragma unroll
    for (int i = 0; i < 8; ++i) { yr[i] = ld_dev(ry, lane * 16, half * 32768 + (wid * 8 + i) * 1024); yi[i] = ld_dev(ry, lane * 16, 65536 + half * 32768 + (wid * 8 + i) * 1024); }
#pragma unroll
    for (int i = 0; i < 16; ++i) { const f32x4 t = acc[i >> 2][i & 3] + bf4(bias[i & 3]);
      const f32x2 a = silu2((f32x2){t[0], t[1]}), b = silu2((f32x2){t[2], t[3]}); acc[i >> 2][i & 3] = (f32x4){a.x, a.y, b.x, b.y}; pinacc(acc[i >> 2][i & 3]); }
    LAS unsigned char* tl = (LAS unsigned char*)shm + wid * (64 * 144);
#pragma unroll
    for (int i = 0; i < 8; ++i) {
      unsigned r[4];
#pragma unroll
      for (int e = 0; e < 4; ++e) { const f32x4 g = acc[i >> 1][(i & 1) * 2 + (e >> 1)];
        r[e] = cvt_pk_bf16((bf_lo(yr[i][e]) - bf_lo(yi[i][e])) * g[(e & 1) * 2], (bf_hi(yr[i][e]) - bf_hi(yi[i][e])) * g[(e & 1) * 2 + 1]); }
      LAS unsigned char* q = tl + ((i >> 1) * 16 + fr) * 144 + (i & 1) * 64 + fq * 8;
      *(LAS u32x2*)q = (u32x2){r[0], r[1]}; *(LAS u32x2*)(q + 32) = (u32x2){r[2], r[3]};
    }
    wait_lgkm0();
#pragma unroll
    for (int j = 0; j < 8; ++j) {
      const u32x4 r = *(const LAS u32x4*)(tl + (j * 8 + (lane >> 3)) * 144 + (lane & 7) * 16);
      u32x4 o;
#pragma unroll
      for (int e = 0; e < 4; ++e) o[e] = cvt_pk_bf16(bf_lo(xr[j][e]) + bf_lo(r[e]), bf_hi(xr[j][e]) + bf_hi(r[e]));
      __builtin_nontemporal_store(o, (u32x4*)(out + xo + (size_t)j * 8 * D_));
    }
    STAMP(1, 9);
  }
}

extern "C" void kernel_launch(void* const* d_in, const int* in_sizes, int n_in, void* d_out, int out_size, void* d_ws, size_t ws_size, hipStream_t stream) {
  const u16* x = (const u16*)d_in[0]; const u16* ws_re = (const u16*)d_in[1]; const u16* ws_im = (const u16*)d_in[2];
  const u16* wd_re = (const u16*)d_in[3]; const u16* wd_im = (const u16*)d_in[4];
  const u16* w1 = (const u16*)d_in[6]; const u16* b1 = (const u16*)d_in[7]; const u16* w2 = (const u16*)d_in[8]; const u16* b2 = (const u16*)d_in[9];
  unsigned char* ws = (unsigned char*)d_ws;
  (void)hipFuncSetAttribute((const void*)k_main, hipFuncAttributeMaxDynamicSharedMemorySize, LDSM);
  k_ln<<<256, 512, 0, stream>>>(x, wd_re, wd_im, w1, b1, b2, ws);
  k_main<<<256, 256, LDSM, stream>>>(x, ws_re, ws_im, wd_re, wd_im, w1, b1, w2, b2, ws, (u16*)d_out);
}
```

```cpp
#include <hip/hip_runtime.h>
#include <hip/hip_bf16.h>
#include <type_traits>
#include <utility>

#define LAS __attribute__((address_space(3)))
#define DI __device__ __forceinline__
typedef unsigned short u16;
typedef short bf16x4 __attribute__((ext_vector_type(4)));
typedef float f32x4 __attribute__((ext_vector_type(4)));
typedef float f32x2 __attribute__((ext_vector_type(2)));
typedef unsigned u32x4 __attribute__((ext_vector_type(4)));
typedef unsigned u32x2 __attribute__((ext_vector_type(2)));

constexpr int S_ = 2048, D_ = 1024, F_ = 3584;
constexpr size_t WS_FLAG = 0;
constexpr int FL_AB = 0, FL_H = 16, FL_W2 = 24, FL_Y = 32, FL_N = 32 + 128;
constexpr size_t WS_XA = 65536;
constexpr size_t WS_AB = WS_XA + (size_t)S_ * D_ * 2;
constexpr size_t WS_H = WS_AB + (size_t)S_ * 2 * D_ * 2;
constexpr size_t WS_W2 = WS_H + (size_t)S_ * F_ * 2;
constexpr size_t WS_PY = WS_W2 + (size_t)F_ * D_ * 2;
constexpr unsigned OOB = 0x40000000u;
constexpr int SPIN_CAP = 1 << 20;
constexpr int W1P = 256;

template <int N> DI void wait_vm() { asm volatile("s_waitcnt vmcnt(%0)" ::"n"(N) : "memory"); }
DI void wait_lgkm0() { asm volatile("s_waitcnt lgkmcnt(0)" ::: "memory"); }
template <int N> DI void wait_lgkm() { asm volatile("s_waitcnt lgkmcnt(%0)" ::"n"(N > 15 ? 15 : N) : "memory"); }
DI void bar() { asm volatile("s_barrier" ::: "memory"); }
DI unsigned cvt_pk_bf16(float lo, float hi) {
  typedef __bf16 bf16x2 __attribute__((ext_vector_type(2)));
  f32x2 v = {lo, hi}; bf16x2 b = __builtin_convertvector(v, bf16x2); return __builtin_bit_cast(unsigned, b);
}
DI float bf_lo(unsigned w) { return __uint_as_float(w << 16); }
DI float bf_hi(unsigned w) { return __uint_as_float(w & 0xffff0000u); }
DI float rbf(float v) { return bf_lo(cvt_pk_bf16(v, v)); }
template <int I, int N, class F> DI void static_for(F&& f) { if constexpr (I < N) { f(std::integral_constant<int, I>{}); static_for<I + 1, N>(f); } }

#define ACLOB "a0","a1","a2","a3","a4","a5","a6","a7","a8","a9","a10","a11","a12","a13","a14","a15","a16","a17","a18","a19","a20","a21","a22","a23","a24","a25","a26","a27","a28","a29","a30","a31","a32","a33","a34","a35","a36","a37","a38","a39","a40","a41","a42","a43","a44","a45","a46","a47","a48","a49","a50","a51","a52","a53","a54","a55","a56","a57","a58","a59","a60","a61","a62","a63","a64","a65","a66","a67","a68","a69","a70","a71","a72","a73","a74","a75","a76","a77","a78","a79","a80","a81","a82","a83","a84","a85","a86","a87","a88","a89","a90","a91","a92","a93","a94","a95","a96","a97","a98","a99","a100","a101","a102","a103","a104","a105","a106","a107","a108","a109","a110","a111","a112","a113","a114","a115","a116","a117","a118","a119","a120","a121","a122","a123","a124","a125","a126","a127"
template <int RA, int RB> DI void mfma16(f32x4& acc) { asm volatile("v_mfma_f32_16x16x32_bf16 %0, a[%c1:%c2], a[%c3:%c4], %0" : "+v"(acc) : "n"(RA), "n"(RA + 3), "n"(RB), "n"(RB + 3)); }
template <int RD, int OFF> DI void ds_r128(unsigned addr) { asm volatile("ds_read_b128 a[%c1:%c2], %0 offset:%c3" ::"v"(addr), "n"(RD), "n"(RD + 3), "n"(OFF) : "memory"); }
template <int RD, int OFF> DI void ds_rtr(unsigned addr) { asm volatile("ds_read_b64_tr_b16 a[%c1:%c2], %0 offset:%c3" ::"v"(addr), "n"(RD), "n"(RD + 1), "n"(OFF) : "memory"); }
DI void wait_frags() { asm volatile("s_waitcnt lgkmcnt(0)" ::: "memory", ACLOB); }
template <int OFF, int POL> DI void dma16(int voff, __amdgpu_buffer_rsrc_t r, unsigned soff) {
  if constexpr (POL == 0) asm volatile("buffer_load_dwordx4 %0, %1, %2 offen offset:%3 lds" ::"v"(voff), "s"(r), "s"(soff), "n"(OFF) : "memory");
  if constexpr (POL == 1) asm volatile("buffer_load_dwordx4 %0, %1, %2 offen offset:%3 sc1 lds" ::"v"(voff), "s"(r), "s"(soff), "n"(OFF) : "memory");
  if constexpr (POL == 2) asm volatile("buffer_load_dwordx4 %0, %1, %2 offen offset:%3 nt lds" ::"v"(voff), "s"(r), "s"(soff), "n"(OFF) : "memory");
}
DI void pinacc(f32x4& v) { asm volatile("" : "+v"(v)); }
template <int C> DI int s_eq(int v) { int r; asm volatile("s_cmp_eq_u32 %1, %2\n\ts_cselect_b32 %0, 1, 0" : "=s"(r) : "s"(v), "n"(C) : "scc"); return r; }
DI __amdgpu_buffer_rsrc_t mkrsrc(const void* p, unsigned bytes) { return __builtin_amdgcn_make_buffer_rsrc((void*)p, 0, bytes, 0x00020000); }

#define STAMP(k, i) do {} while (0)
#define STAGGER 0
#define SNAKE 1
enum { ROWK = 0, FRAG = 1, NMAJ = 2 };

template <int KIND, int R, int POL> struct Stream {
  static constexpr bool ODD = KIND == NMAJ && R == 224;
  static constexpr int P = R / 32, NG = (P + 3) / 4, NV = ODD ? 7 : (KIND == FRAG ? 2 : 4);
  __amdgpu_buffer_rsrc_t rs;
  int voff[NV];
  unsigned s0[NG], ks;
  unsigned ml[NG];
  static DI int gpiece(int g, int wid) { if constexpr (ODD) return wid * 7 + 4 * g; else return 16 * g + 4 * wid; }
  DI void init_rowk(const void* p, unsigned bytes, int ld, int r0, int k0, int wid, int lane) {
    rs = mkrsrc(p, bytes);
#pragma unroll
    for (int j = 0; j < 4; ++j) voff[j] = ((lane >> 3) + 8 * j) * ld * 2 + (((lane & 7) ^ ((4 * (j & 1) + (lane >> 4)) & 7)) * 16) - j * 1024;
#pragma unroll
    for (int g = 0; g < NG; ++g) s0[g] = (unsigned)((r0 + 8 * gpiece(g, wid)) * ld + k0) * 2u;
    ks = 128u;
  }
  DI void init_frag(const void* p, unsigned bytes, int kbn, int r0, int k0, int wid, int lane) {
    rs = mkrsrc(p, bytes);
    voff[0] = lane * 16; voff[1] = lane * 16 + kbn * 1024 - 2048;
#pragma unroll
    for (int g = 0; g < NG; ++g) s0[g] = (unsigned)((r0 / 16 + gpiece(g, wid) / 2) * kbn + k0 / 32) * 1024u;
    ks = 2048u;
  }
  DI void init_nmaj(const void* p, unsigned bytes, int ld, int n0, int k0, int wid, int lane) {
    rs = mkrsrc(p, bytes);
    if constexpr (ODD) {
#pragma unroll
      for (int j = 0; j < 7; ++j) { const int L = (wid * 7 + j) * 64 + lane, kk = L / 28, pos = L % 28;
        voff[j] = kk * ld * 2 + (pos ^ (((kk >> 3) & 1) << 1)) * 16 - (j & 3) * 1024; }
      s0[0] = s0[1] = (unsigned)(k0 * ld + n0) * 2u;
    } else {
      static_assert(R == 128 || ODD, "");
#pragma unroll
      for (int j = 0; j < 4; ++j) { const int kk = 4 * (4 * wid + j) + (lane >> 4), g = (kk & 3) | (((kk >> 3) & 1) << 2);
        voff[j] = (4 * j + (lane >> 4)) * ld * 2 + (((lane & 15) ^ (g << 1)) * 16) - j * 1024; }
      s0[0] = (unsigned)((k0 + 16 * wid) * ld + n0) * 2u;
    }
    ks = (unsigned)(64 * ld) * 2u;
  }
  DI void init_lds(unsigned base, int wid) {
#pragma unroll
    for (int g = 0; g < NG; ++g) ml[g] = base + (unsigned)gpiece(g, wid) * 1024u;
  }
  template <int I, int SO> DI void m0() const { asm volatile("s_add_u32 m0, %0, %1" ::"s"(ml[I / 4]), "n"(SO) : "memory", "scc"); }
  template <int I> DI void issue() const {
    constexpr int g = I / 4, j = I % 4, vi = ODD ? I : (KIND == FRAG ? (j >> 1) : j);
    dma16<j * 1024, POL>(voff[vi], rs, s0[g]);
  }
  template <int I> DI void advance() { asm volatile("s_add_u32 %0, %0, %1" : "+s"(s0[I / 4]) : "s"(ks) : "scc"); }
};

template <int KIND, int R, int T, int NS, int STAGE> struct Reader {
  static constexpr int NA = KIND == FRAG ? 1 : (KIND == ROWK ? 2 : 3);
  static constexpr int SPAN = KIND == NMAJ ? 64 * R * 2 : T * 2048, SPW = (65536 - SPAN) / STAGE + 1, NW = (NS + SPW - 1) / SPW;
  unsigned a[NW][NA];
  DI void init(unsigned base, int t0, int lane) {
    const int fr = lane & 15, fq = lane >> 4;
    if constexpr (KIND == ROWK) { a[0][0] = base + (t0 * 16 + fr) * 128 + ((fq ^ (fr >> 1)) * 16); a[0][1] = base + (t0 * 16 + fr) * 128 + (((4 + fq) ^ (fr >> 1)) * 16); }
    else if constexpr (KIND == FRAG) a[0][0] = base + t0 * 2048 + lane * 16;
    else { static_assert(KIND != NMAJ || R == 224, "");
      const int q = fr >> 2, p = lane & 3, wn = t0 / 7; const unsigned b = base + (8 * fq + q) * 448 + 8 * p;
      a[0][0] = b + 32 * ((6 * wn) ^ (fq & 1)); a[0][1] = b + 32 * ((6 * wn + 1) ^ (fq & 1)); a[0][2] = b + 32 * ((12 + wn) ^ (fq & 1)); }
#pragma unroll
    for (int w = 1; w < NW; ++w)
#pragma unroll
      for (int n = 0; n < NA; ++n) { a[w][n] = a[0][n] + w * SPW * STAGE; asm volatile("" : "+v"(a[w][n])); }
  }
  template <int ST, int KS, int I, int RB> DI void rd() const {
    constexpr int w = ST / SPW, so = (ST % SPW) * STAGE;
    if constexpr (KIND == ROWK) ds_r128<RB + 4 * I, so + I * 2048>(a[w][KS]);
    else if constexpr (KIND == FRAG) ds_r128<RB + 4 * I, so + I * 2048 + KS * 1024>(a[w][0]);
    else { constexpr int n = I / 2, h = I % 2; ds_rtr<RB + 4 * n + 2 * h, so + (32 * KS + 4 * h) * (R * 2) + (n < 6 ? 32 * (n & ~1) : 0)>(a[w][n < 6 ? (n & 1) : 2]); }
  }
};

template <int BM_, int BN_, int NS_, int KA_, int KB_, int PLA_, int PLB_, bool TR_, int NX_, bool LAZY_, int RSP_> struct Geo {
  static constexpr bool LAZY = LAZY_; static constexpr int NX = NX_, PLA = PLA_, PLB = PLB_;
  static constexpr int BM = BM_, BN = BN_, NS = NS_, KA = KA_, KB = KB_, MT = BM / 32, NT = BN / 32, NM = MT * NT;
  static constexpr bool TR = TR_;
  static constexpr int PA = BM / 32, PB = BN / 32, ND = PA + PB, STAGE = (BM + BN) * 128;
  static constexpr int DEPTH = LAZY ? NS - 1 : NS;
  static constexpr int H = (DEPTH == 2) ? ND : ND / 2;
  static constexpr int NIA = KA == NMAJ ? 2 * MT : MT, NIB = KB == NMAJ ? 2 * NT : NT, NR = NIA + NIB, RSPAN = RSP_;
  static constexpr int rgap(int r) { return (r * RSPAN) / NR; }
  static constexpr int rbefore(int i) { int c = 0; for (int r = 0; r < NR; ++r) c += rgap(r) < i; return c; }
  typedef Reader<KA, BM, MT, NS, STAGE> RA; typedef Reader<KB, BN, NT, NS, STAGE> RB;
};
struct Side { __amdgpu_buffer_rsrc_t rs; unsigned voff, step, soff; };
template <class G> struct Pipe {
  Stream<G::KA, G::BM, G::PLA> A; Stream<G::KB, G::BN, G::PLB> B;
  Side xl, x2;
  unsigned ls = 0;
  unsigned* sig = nullptr;
  unsigned lds; int wid;
  DI void side() {
    if constexpr (G::NX > 0) asm volatile("buffer_load_dword a124, %0, %1, %2 offen" ::"v"(xl.voff), "s"(xl.rs), "s"(xl.soff) : "memory");
    if constexpr (G::NX > 1) asm volatile("buffer_load_dword a124, %0, %1, %2 offen" ::"v"(x2.voff), "s"(x2.rs), "s"(x2.soff) : "memory"); }
  DI void side_adv() {
    if constexpr (G::NX > 0) asm volatile("s_add_u32 %0, %0, %1" : "+s"(xl.soff) : "s"(xl.step) : "scc");
    if constexpr (G::NX > 1) asm volatile("s_add_u32 %0, %0, %1" : "+s"(x2.soff) : "s"(x2.step) : "scc"); }
  static constexpr bool gstart(int i) { return i < G::PA ? (i % 4 == 0) : ((i - G::PA) % 4 == 0); }
  static constexpr bool gend(int i) { return i == G::PA - 1 || i == G::ND - 1 || gstart(i + 1); }
  template <int I, int ST> DI void m0() const { if constexpr (I < G::PA) A.template m0<I, ST * G::STAGE>(); else B.template m0<I - G::PA, ST * G::STAGE>(); }
  template <int I> DI void issue() const { if constexpr (I < G::PA) A.template issue<I>(); else B.template issue<I - G::PA>(); }
  template <int I> DI void advance() { if constexpr (I < G::PA) A.template advance<I>(); else B.template advance<I - G::PA>(); }
  template <int MASK> DI void prologue() {
    if constexpr (MASK & 1) A.init_lds(lds, wid);
    if constexpr (MASK & 2) B.init_lds(lds + G::BM * 128, wid);
    asm volatile("s_nop 4" ::: "memory", ACLOB);
    static_for<0, G::DEPTH>([&](auto Ss) {
      if constexpr (MASK == 3 && Ss.value >= 2) { side(); side_adv(); }
      static_for<0, (Ss.value == G::DEPTH - 1 ? G::H : G::ND)>([&](auto Dd) { constexpr int d = Dd.value;
        if constexpr ((MASK >> (d < G::PA ? 0 : 1)) & 1) {
          if constexpr (gstart(d)) { m0<d, Ss.value>(); asm volatile("s_nop 0"); }
          issue<d>();
          if constexpr (gend(d)) advance<d>(); } }); });
  }
};

constexpr int dgap(int d, int ndp, int nm, int w) { return 1 + (d * (nm - 2) + w * (nm - 2) / 4) / ndp; }
constexpr int dat(int i, int ndp, int nm, int w) { for (int d = 0; d < ndp; ++d) if (dgap(d, ndp, nm, w) == i) return d; return -1; }
template <class G, int W, int CUR, int RST, int RKS, int D0, int D1, int DST, int I>
DI void gap(Pipe<G>& p, const typename G::RA& ra, const typename G::RB& rb, f32x4 (&acc)[G::MT][G::NT]) {
  constexpr int CA = 64 * CUR, CB = CA + 32, NA = 64 * (CUR ^ 1), NB = NA + 32;
  constexpr int NM = G::NM, NIB = G::NIB, NR = G::NR, NDP = D1 - D0, m = I / G::NT, n0 = I % G::NT, n = (SNAKE && (m & 1)) ? G::NT - 1 - n0 : n0;
  constexpr int d = dat(I, NDP, NM, W), dp = dat(I - 1, NDP, NM, W);
  if constexpr (n0 == 0 && m % 2 == 0 && (G::LAZY || CUR == 0)) wait_lgkm<(NR - 1 - (NIB + m + 1)) + G::rbefore(I)>();
  if constexpr (d >= 0) if constexpr (Pipe<G>::gstart(D0 + d)) p.template m0<D0 + d, DST>();
  if constexpr (G::TR) mfma16<CB + 4 * n, CA + 4 * m>(acc[m][n]); else mfma16<CA + 4 * m, CB + 4 * n>(acc[m][n]);
  if constexpr (I == 0 && CUR == (G::DEPTH == 2 ? 0 : 1)) p.side();
  if constexpr (I == NM - 1 && CUR == (G::DEPTH == 2 ? 0 : 1)) p.side_adv();
  if constexpr (dp >= 0) if constexpr (Pipe<G>::gend(D0 + dp)) p.template advance<D0 + dp>();
  if constexpr (d >= 0) p.template issue<D0 + d>();
  static_for<G::rbefore(I), G::rbefore(I + 1)>([&](auto Rr) { constexpr int r = Rr.value;
    if constexpr (r == 0) ra.template rd<RST, RKS, 0, NA>();
    else if constexpr (r <= NIB) rb.template rd<RST, RKS, r - 1, NB>();
    else ra.template rd<RST, RKS, r - NIB, NA>(); });
}
template <class G, int W, int CUR, int RST, int RKS, int D0, int D1, int DST, int... Is>
DI void phase_(Pipe<G>& p, const typename G::RA& ra, const typename G::RB& rb, f32x4 (&acc)[G::MT][G::NT], std::integer_sequence<int, Is...>) {
  (gap<G, W, CUR, RST, RKS, D0, D1, DST, Is>(p, ra, rb, acc), ...);
}
template <class G, int W, int ST>
DI void ktile(Pipe<G>& p, const typename G::RA& ra, const typename G::RB& rb, f32x4 (&acc)[G::MT][G::NT]) {
  constexpr int NS = G::NS, ND = G::ND, H = G::H, DP = G::DEPTH;
  phase_<G, W, 0, ST, 1, H, ND, (ST + DP - 1) % NS>(p, ra, rb, acc, std::make_integer_sequence<int, G::NM>{});
  if constexpr (G::LAZY) asm volatile("s_waitcnt vmcnt(%0)\n\ts_barrier" ::"n"((DP - 2) * (ND + G::NX)) : "memory", ACLOB);
  else asm volatile("s_waitcnt vmcnt(%0) lgkmcnt(0)\n\ts_barrier" ::"n"(DP == 2 ? G::NX : (DP - 2) * (ND + G::NX)) : "memory", ACLOB);
  phase_<G, W, 1, (ST + 1) % NS, 0, 0, H, (ST + DP) % NS>(p, ra, rb, acc, std::make_integer_sequence<int, G::NM>{});
}
template <class G, int W, int NTILE>
DI void gemm_body(Pipe<G>& p, const typename G::RA& ra, const typename G::RB& rb, f32x4 (&acc)[G::MT][G::NT], int on) {
#pragma nounroll
  for (int it = on * (NTILE / G::NS); it > 0; --it) { static_for<0, G::NS>([&](auto Ss) { ktile<G, W, Ss.value>(p, ra, rb, acc); });
  }
  if constexpr (NTILE % G::NS != 0) {
    asm volatile("" : "+s"(on));
#pragma nounroll
    for (int it = on; it > 0; --it) static_for<0, NTILE % G::NS>([&](auto Ss) { ktile<G, W, Ss.value>(p, ra, rb, acc); });
  }
}
template <class G, int NTILE, bool SPLIT, class Init>
DI void gemm_loop(Pipe<G>& p, int lane, f32x4 (&acc)[G::MT][G::NT], int st0, Init init) {
  constexpr int ND = G::ND, H = G::H, DP = G::DEPTH;
  typename G::RA ra; typename G::RB rb;
  ra.init(p.lds, (p.wid >> 1) * G::MT, lane); rb.init(p.lds + G::BM * 128, (p.wid & 1) * G::NT, lane);
#pragma unroll
  for (int n = 0; n < G::NT; ++n) { const f32x4 v = init(n);
#pragma unroll
    for (int m = 0; m < G::MT; ++m) { acc[m][n] = v; pinacc(acc[m][n]); } }
  if constexpr (SPLIT && H < ND) if constexpr (!Pipe<G>::gstart(H)) p.template m0<H, DP - 1>();
  wait_vm<(SPLIT ? 0 : (DP - 2) * (ND + G::NX) + H)>();
  bar();
  if (p.sig != nullptr && threadIdx.x == 0) __hip_atomic_fetch_add(p.sig, 1u, __ATOMIC_RELAXED, __HIP_MEMORY_SCOPE_AGENT);
  static_for<0, G::NIA>([&](auto I) { ra.template rd<0, 0, I.value, 0>(); });
  static_for<0, G::NIB>([&](auto I) { rb.template rd<0, 0, I.value, 32>(); });
  wait_frags();
  STAMP(1, st0);
  asm volatile("s_nop 1");
  gemm_body<G, 0, NTILE>(p, ra, rb, acc, 1);
  asm volatile("s_nop 7\n\ts_nop 7\n\ts_waitcnt vmcnt(0) lgkmcnt(0)" ::: "memory", ACLOB);
#pragma unroll
  for (int m = 0; m < G::MT; ++m)
#pragma unroll
    for (int n = 0; n < G::NT; ++n) pinacc(acc[m][n]);
  STAMP(1, st0 + 1);
}

DI void touch(unsigned& sink, const void* p) { asm volatile("global_load_dword %0, %1, off" : "+v"(sink) : "v"(p)); }
DI float wave_sum(float a) {
#pragma unroll
  for (int o = 1; o < 64; o <<= 1) a += __shfl_xor(a, o);
  return a;
}
__global__ __launch_bounds__(512) void k_ln(const u16* __restrict__ x, const u16* __restrict__ wd_re, const u16* __restrict__ wd_im, const u16* __restrict__ w1,
                                            const u16* __restrict__ b1, const u16* __restrict__ b2, unsigned char* __restrict__ ws) {
  __shared__ __attribute__((aligned(16))) unsigned char tl[128 * 144];
  const int tid = threadIdx.x, lane = tid & 63, w = tid >> 6, r0 = blockIdx.x * 8;
  STAMP(0, 0);
  u32x4 raw[2];
  { const u16* px = x + (size_t)(r0 + w) * D_ + lane * 8;
    asm volatile("global_load_dwordx4 %0, %2, off nt\n\tglobal_load_dwordx4 %1, %2, off offset:1024 nt" : "=&v"(raw[0]), "=&v"(raw[1]) : "v"(px)); }
  unsigned sink = 0;
  { const int g = tid * 256 + blockIdx.x;
    const int l = g - W1P * 56;
    const char* p = l < 0 ? (const char*)w1 + (size_t)g * 128 : (l < 32768 ? (const char*)(l < 16384 ? wd_re : wd_im) + (size_t)(l & 16383) * 128 : ((l & 127) < 56 ? (const char*)b1 + (l & 127) * 128 : (const char*)b2 + (l & 15) * 128));
    asm volatile("global_load_dword %0, %1, off" : "+v"(sink) : "v"(p)); }
  asm volatile("s_waitcnt vmcnt(1)" : "+v"(raw[0]), "+v"(raw[1]));
  float v[16];
#pragma unroll
  for (int j = 0; j < 2; ++j)
#pragma unroll
    for (int e = 0; e < 4; ++e) { v[8 * j + 2 * e] = bf_lo(raw[j][e]); v[8 * j + 2 * e + 1] = bf_hi(raw[j][e]); }
  float a = 0.f;
#pragma unroll
  for (int e = 0; e < 16; ++e) a += v[e];
  const float mean = wave_sum(a) * (1.0f / D_);
  a = 0.f;
#pragma unroll
  for (int e = 0; e < 16; ++e) { const float dd = v[e] - mean; a += dd * dd; }
  const float rden = 1.0f / rbf(sqrtf(rbf(rbf(wave_sum(a) * (1.0f / D_)) + 1e-5f))), mb = rbf(mean);
#pragma unroll
  for (int j = 0; j < 2; ++j) {
    u32x4 o;
#pragma unroll
    for (int e = 0; e < 4; ++e) o[e] = cvt_pk_bf16(rbf(v[8 * j + 2 * e] - mb) * rden, rbf(v[8 * j + 2 * e + 1] - mb) * rden);
    *(u32x4*)(tl + (j * 64 + lane) * 144 + w * 16) = o;
  }
  wait_lgkm0(); bar();
  unsigned char* xa = ws + WS_XA + (size_t)(r0 >> 4) * 32768 + (r0 & 8) * 16;
#pragma unroll
  for (int j = 0; j < 2; ++j) { const int ch = j * 64 + (tid >> 3);
    *(u32x4*)(xa + ch * 256 + (tid & 7) * 16) = *(const u32x4*)(tl + ch * 144 + (tid & 7) * 16); }
  if (tid == 0 && blockIdx.x < FL_N) *(unsigned*)(ws + WS_FLAG + (size_t)blockIdx.x * 128) = 0u;
  asm volatile("s_waitcnt vmcnt(0)" : "+v"(sink));
  STAMP(0, 3);
}

DI f32x2 sigm_mul(f32x2 x, f32x2 arg) {
  f32x2 e; e.x = __builtin_amdgcn_exp2f(arg.x); e.y = __builtin_amdgcn_exp2f(arg.y);
  const f32x2 d = e + 1.0f;
  f32x2 r; r.x = __builtin_amdgcn_rcpf(d.x); r.y = __builtin_amdgcn_rcpf(d.y);
  return x * r;
}
DI f32x2 gelu2(f32x2 x) {
  const float k0 = -2.0f * 0.7978845608028654f * 1.4426950408889634f, k1 = k0 * 0.044715f;
  return sigm_mul(x, x * ((x * x) * k1 + k0));
}
DI f32x2 silu2(f32x2 x) { return sigm_mul(x, x * -1.4426950408889634f); }
DI f32x4 gelu4(f32x4 t) { const f32x2 a = gelu2((f32x2){t[0], t[1]}), b = gelu2((f32x2){t[2], t[3]}); return (f32x4){a.x, a.y, b.x, b.y}; }
DI f32x4 bf4(u32x2 w) { return (f32x4){bf_lo(w[0]), bf_hi(w[0]), bf_lo(w[1]), bf_hi(w[1])}; }

#define GO_NS 4
#define GO_LAZY false
#define GO_RSP 11
#define DEVPOL 1
typedef Geo<256, 128, 3, FRAG, ROWK, 0, 0, false, 1, false, 24> GAB;
#define YT1 3
#define YT2 11
#define ABT 4
typedef Geo<256, 128, 3, ROWK, FRAG, 0, DEVPOL, true, 0, false, 24> GY;
#define H_TOUCH (ABT < 4)
typedef Geo<256, 224, 2, FRAG, NMAJ, 0, 0, true, H_TOUCH, false, 42> GH;
typedef Geo<128, 128, GO_NS, FRAG, FRAG, DEVPOL, DEVPOL, true, 0, GO_LAZY, GO_RSP> GO;
constexpr int LDSM = GY::NS * GY::STAGE > GO::NS * GO::STAGE ? GY::NS * GY::STAGE : GO::NS * GO::STAGE;

DI u32x4 pack_pair(const f32x4& a, const f32x4& b) {
  const unsigned a0 = cvt_pk_bf16(a[0], a[1]), a1 = cvt_pk_bf16(a[2], a[3]), b0 = cvt_pk_bf16(b[0], b[1]), b1 = cvt_pk_bf16(b[2], b[3]);
  const auto r0 = __builtin_amdgcn_permlane16_swap(a0, b0, false, false);
  const auto r1 = __builtin_amdgcn_permlane16_swap(a1, b1, false, false);
  return (u32x4){r0[0], r1[0], r0[1], r1[1]};
}
DI u32x4 pack8(const f32x4& a, const f32x4& b) { return (u32x4){cvt_pk_bf16(a[0], a[1]), cvt_pk_bf16(a[2], a[3]), cvt_pk_bf16(b[0], b[1]), cvt_pk_bf16(b[2], b[3])}; }
struct ZeroInit { DI f32x4 operator()(int) const { return (f32x4){0.f, 0.f, 0.f, 0.f}; } };
DI void wait_bias(u32x2& b) { asm volatile("s_waitcnt vmcnt(%1)" : "+v"(b) : "n"(2 * GH::ND)); }

DI void st_dev(__amdgpu_buffer_rsrc_t r, unsigned off, u32x4 v) { __builtin_amdgcn_raw_buffer_store_b128(v, r, off, 0, 16); }
DI void st_dev8(__amdgpu_buffer_rsrc_t r, unsigned off, u32x2 v) { __builtin_amdgcn_raw_buffer_store_b64(v, r, off, 0, 16); }
DI u32x4 ld_dev(__amdgpu_buffer_rsrc_t r, unsigned voff, unsigned soff) { return __builtin_amdgcn_raw_buffer_load_b128(r, voff, soff, 16); }
DI void publish(unsigned* cnt) {
  wait_vm<0>();
  bar();
  if (threadIdx.x == 0) __hip_atomic_fetch_add(cnt, 1u, __ATOMIC_RELAXED, __HIP_MEMORY_SCOPE_AGENT);
}
DI void poll(unsigned* f, unsigned want) {
#pragma nounroll
  for (int i = 0; i < SPIN_CAP && __hip_atomic_load(f, __ATOMIC_RELAXED, __HIP_MEMORY_SCOPE_AGENT) != want; ++i) __builtin_amdgcn_s_sleep(1); }
DI void await1(unsigned* f0, unsigned w0) {
  if (threadIdx.x == 0) poll(f0, w0);
  asm volatile("" ::: "memory");
  bar();
}
DI void await2(unsigned* f0, unsigned w0, unsigned* f1, unsigned w1) {
  if (threadIdx.x == 0) { poll(f0, w0); poll(f1, w1); }
  asm volatile("" ::: "memory");
  bar();
}

__global__ __launch_bounds__(256, 1) void k_main(const u16* __restrict__ x, const u16* __restrict__ ws_re, const u16* __restrict__ ws_im, const u16* __restrict__ wd_re, const u16* __restrict__ wd_im,
                                                 const u16* __restrict__ w1, const u16* __restrict__ b1, const u16* __restrict__ w2, const u16* __restrict__ b2, unsigned char* ws, u16* __restrict__ out) {
  extern __shared__ __attribute__((aligned(16))) unsigned char shm[];
  const char* pc; asm volatile("s_getpc_b64 %0" : "=s"(pc));
  const int wid = __builtin_amdgcn_readfirstlane(threadIdx.x >> 6), lane = threadIdx.x & 63, fr = lane & 15, fq = lane >> 4, wm = wid >> 1, wn = wid & 1;
  const int xcd = blockIdx.x & 7, slot = blockIdx.x >> 3, chain = slot >> 4, s4 = slot & 15;
  const int pm1 = (xcd & 1) * 4 + (s4 & 3), pn1 = (xcd >> 1) * 4 + (s4 >> 2);
  const int half = xcd & 1, pm = (xcd >> 2) * 4 + (s4 & 3), pn = ((xcd >> 1) & 1) * 4 + (s4 >> 2), tile = pm * 8 + pn;
  const unsigned ldsb = (unsigned)(size_t)(LAS unsigned char*)shm;
  const int lo = ((fq & 1) * 2 + (fq >> 1)) * 256 + fr * 16;
  unsigned* flags = (unsigned*)(ws + WS_FLAG);
  const __amdgpu_buffer_rsrc_t ry = mkrsrc(ws + WS_PY + (size_t)tile * 131072, 131072);
  STAMP(1, 0);
  if (chain == 0) {
    const int kb = xcd * 16 + s4, yt = kb * 256 + threadIdx.x;
    const u16* wsm = half ? ws_im : ws_re;
    const u16* wshare = wsm + (size_t)(pm * 256 + pn * 32) * S_;
    unsigned sink = 0;
    {
      if (s4 == 0) touch(sink, pc + threadIdx.x * 128);
      touch(sink, (const char*)(w1 + (size_t)W1P * F_) + (size_t)yt * 128);
      if (yt + 32768 < (D_ - W1P) * 56) touch(sink, (const char*)(w1 + (size_t)W1P * F_) + (size_t)(yt + 32768) * 128);
      if (kb < 112) {
        const __amdgpu_buffer_rsrc_t rs = mkrsrc(w2 + (size_t)kb * 32 * D_, 32 * D_ * 2);
        asm volatile("s_nop 4" ::: "memory");
        static_for<0, 16>([&](auto I) { constexpr int i = I.value;
          const int g = ((i >> 1) & 3) | ((wid & 1) << 2);
          asm volatile("s_add_u32 m0, %0, %1\n\ts_nop 0" ::"s"(ldsb + wid * 16384), "n"(i * 1024) : "memory", "scc");
          dma16<0, 2>((lane ^ (g << 1)) * 16, rs, wid * 16384 + i * 1024); });
        wait_vm<0>(); bar();
        const int q = fr >> 2, gk = q | ((fq & 1) << 2);
        const __amdgpu_buffer_rsrc_t rw = mkrsrc(ws + WS_W2, F_ * D_ * 2);
        LAS unsigned char* src = (LAS unsigned char*)shm + (8 * fq + q) * 2048 + wid * 512 + 8 * (lane & 3);
#pragma unroll
        for (int n = 0; n < 16; ++n) {
          const bf16x4 l4 = __builtin_amdgcn_ds_read_tr16_b64_v4i16((LAS bf16x4*)(src + 32 * (n ^ gk)));
          const bf16x4 h4 = __builtin_amdgcn_ds_read_tr16_b64_v4i16((LAS bf16x4*)(src + 32 * (n ^ gk) + 8192));
          const u32x2 a = __builtin_bit_cast(u32x2, l4), b = __builtin_bit_cast(u32x2, h4);
          st_dev(rw, (unsigned)((wid * 16 + n) * 112 + kb) * 1024u + lane * 16, (u32x4){a[0], a[1], b[0], b[1]});
        }
        wait_lgkm0(); bar();
      }
      STAMP(1, 4);
    }
    f32x4 acc[8][4];
    {
      Pipe<GAB> p; p.lds = ldsb; p.wid = wid;
      p.xl.rs = mkrsrc(wshare + (size_t)wid * 8 * S_, ABT * 8192); p.xl.step = 8192; p.xl.soff = 0u - (unsigned)(kb % (17 - ABT)) * 8192u; p.xl.voff = lane * 128;
      p.A.init_frag(ws + WS_XA, S_ * D_ * 2, 32, pm1 * 256, 0, wid, lane);
      p.B.init_rowk(pn1 < 8 ? wd_re : wd_im, D_ * D_ * 2, D_, (pn1 & 7) * 128, 0, wid, lane);
      p.prologue<3>();
      p.sig = flags + FL_W2 * 32;
      gemm_loop<GAB, 16, false>(p, lane, acc, 1, ZeroInit());
      const __amdgpu_buffer_rsrc_t ro = mkrsrc(ws + WS_AB, S_ * 2 * D_ * 2);
      const unsigned ob = (unsigned)((pn1 * 8 + wn * 4) * 64 + pm1 * 8 + wm * 4) * 1024u + lo;
#pragma unroll
      for (int n = 0; n < 4; ++n)
#pragma unroll
        for (int m = 0; m < 8; m += 2) st_dev(ro, ob + (n * 64 + m / 2) * 1024, pack_pair(acc[m][n], acc[m + 1][n]));
      publish(flags + (FL_AB + pn1) * 32);
      STAMP(1, 3);
    }
    {
      Pipe<GY> p; p.lds = ldsb; p.wid = wid;
      p.A.init_rowk(wsm, S_ * S_ * 2, S_, pm * 256, 0, wid, lane);
      p.B.init_frag(ws + WS_AB, S_ * 2 * D_ * 2, 64, half * D_ + pn * 128, 0, wid, lane);
      p.prologue<1>();
      await1(flags + (FL_AB + half * 8 + pn) * 32, 8);
      STAMP(1, 5);
      p.prologue<2>();
      gemm_loop<GY, 32, true>(p, lane, acc, 6, ZeroInit());
      asm volatile("" : "+v"(sink));
#pragma unroll
      for (int i = 0; i < 16; ++i) st_dev(ry, lane * 16 + half * 65536 + wm * 32768 + (((i >> 3) * 2 + wn) * 8 + (i & 7)) * 1024, pack8(acc[i >> 1][(i & 1) * 2], acc[i >> 1][(i & 1) * 2 + 1]));
      publish(flags + (FL_Y + tile * 2 + half) * 32);
      STAMP(1, 8);
    }
  } else {
    {
      const int c0 = pn1 * 224;
      Pipe<GH> p; p.lds = ldsb; p.wid = wid;
      p.xl.rs = mkrsrc((const char*)((half ? ws_im : ws_re) + (size_t)(pm * 256 + pn * 32 + wid * 8) * S_) + ABT * 8192, (4 - ABT) * 8192); p.xl.step = 8192;
      p.xl.soff = 0u - (unsigned)(2 + (xcd * 16 + s4) % 11) * 8192u; p.xl.voff = lane * 128;
      u32x2 bias[7];
#pragma unroll
      for (int n = 0; n < 7; ++n) asm volatile("global_load_dwordx2 %0, %1, off" : "=v"(bias[n]) : "v"(b1 + c0 + (n < 6 ? wn * 96 + n * 16 : 192 + wn * 16) + fq * 4));
      p.A.init_frag(ws + WS_XA, S_ * D_ * 2, 32, pm1 * 256, 0, wid, lane);
      p.B.init_nmaj(w1, D_ * F_ * 2, F_, c0, 0, wid, lane);
      p.prologue<3>();
      f32x4 acc[8][7];
      gemm_loop<GH, 16, false>(p, lane, acc, 1, [&](int n) { wait_bias(bias[n]); return bf4(bias[n]); });
      const __amdgpu_buffer_rsrc_t ro = mkrsrc(ws + WS_H, S_ * F_ * 2);
      const unsigned ob = (unsigned)((pm1 * 16 + wm * 8) * 112 + pn1 * 7) * 1024u, op = ob + wn * 3072 + lo, os = ob + 6144 + (wn * 2 + (fq >> 1)) * 256 + fr * 16 + (fq & 1) * 8;
#pragma unroll
      for (int m = 0; m < 8; ++m) {
#pragma unroll
        for (int n = 0; n < 6; n += 2) st_dev(ro, op + (m * 112 + n / 2) * 1024, pack_pair(gelu4(acc[m][n]), gelu4(acc[m][n + 1])));
        const f32x4 g = gelu4(acc[m][6]); st_dev8(ro, os + m * 112 * 1024, (u32x2){cvt_pk_bf16(g[0], g[1]), cvt_pk_bf16(g[2], g[3])});
      }
      STAMP(1, 3);
      publish(flags + (FL_H + pm1) * 32);
      STAMP(1, 4);
    }
    f32x4 acc[4][4];
    Pipe<GO> p; p.lds = ldsb; p.wid = wid;
    p.A.init_frag(ws + WS_H, S_ * F_ * 2, 112, pm * 256 + half * 128, 0, wid, lane);
    p.B.init_frag(ws + WS_W2, F_ * D_ * 2, 112, pn * 128, 0, wid, lane);
    u32x2 bias[4]; u32x4 xr[8];
    { const size_t xo = (size_t)(pm * 256 + half * 128 + wm * 64 + (lane >> 3)) * D_ + pn * 128 + wn * 64 + (lane & 7) * 8;
#pragma unroll
      for (int n = 0; n < 4; ++n) asm volatile("global_load_dwordx2 %0, %1, off" : "=v"(bias[n]) : "v"(b2 + pn * 128 + wn * 64 + n * 16 + fq * 4));
#pragma unroll
      for (int j = 0; j < 8; ++j) asm volatile("global_load_dwordx4 %0, %1, off nt" : "=v"(xr[j]) : "v"(x + xo + (size_t)j * 8 * D_)); }
    await1(flags + FL_W2 * 32, 128);
    p.prologue<2>();
    await1(flags + (FL_H + pm) * 32, 16);
    STAMP(1, 5);
    p.prologue<1>();
    const size_t xo = (size_t)(pm * 256 + half * 128 + wm * 64 + (lane >> 3)) * D_ + pn * 128 + wn * 64 + (lane & 7) * 8;
    gemm_loop<GO, 56, true>(p, lane, acc, 6, ZeroInit());
    await2(flags + (FL_Y + tile * 2) * 32, 1, flags + (FL_Y + tile * 2 + 1) * 32, 1);
    STAMP(1, 8);
    u32x4 yr[8], yi[8];
#pragma unroll
    for (int j = 0; j < 8; ++j) asm volatile("" : "+v"(xr[j]));
#pragma unroll
    for (int n = 0; n < 4; ++n) asm volatile("" : "+v"(bias[n]));
#pragma unroll
    for (int i = 0; i < 8; ++i) { yr[i] = ld_dev(ry, lane * 16, half * 32768 + (wid * 8 + i) * 1024); yi[i] = ld_dev(ry, lane * 16, 65536 + half * 32768 + (wid * 8 + i) * 1024); }
#pragma unroll
    for (int i = 0; i < 16; ++i) { const f32x4 t = acc[i >> 2][i & 3] + bf4(bias[i & 3]);
      const f32x2 a = silu2((f32x2){t[0], t[1]}), b = silu2((f32x2){t[2], t[3]}); acc[i >> 2][i & 3] = (f32x4){a.x, a.y, b.x, b.y}; pinacc(acc[i >> 2][i & 3]); }
    LAS unsigned char* tl = (LAS unsigned char*)shm + wid * (64 * 144);
#pragma unroll
    for (int i = 0; i < 8; ++i) {
      unsigned r[4];
#pragma unroll
      for (int e = 0; e < 4; ++e) { const f32x4 g = acc[i >> 1][(i & 1) * 2 + (e >> 1)];
        r[e] = cvt_pk_bf16((bf_lo(yr[i][e]) - bf_lo(yi[i][e])) * g[(e & 1) * 2], (bf_hi(yr[i][e]) - bf_hi(yi[i][e])) * g[(e & 1) * 2 + 1]); }
      LAS unsigned char* q = tl + ((i >> 1) * 16 + fr) * 144 + (i & 1) * 64 + fq * 8;
      *(LAS u32x2*)q = (u32x2){r[0], r[1]}; *(LAS u32x2*)(q + 32) = (u32x2){r[2], r[3]};
    }
    wait_lgkm0();
#pragma unroll
    for (int j = 0; j < 8; ++j) {
      const u32x4 r = *(const LAS u32x4*)(tl + (j * 8 + (lane >> 3)) * 144 + (lane & 7) * 16);
      u32x4 o;
#pragma unroll
      for (int e = 0; e < 4; ++e) o[e] = cvt_pk_bf16(bf_lo(xr[j][e]) + bf_lo(r[e]), bf_hi(xr[j][e]) + bf_hi(r[e]));
      __builtin_nontemporal_store(o, (u32x4*)(out + xo + (size_t)j * 8 * D_));
    }
    STAMP(1, 9);
  }
}

extern "C" void kernel_launch(void* const* d_in, const int* in_sizes, int n_in, void* d_out, int out_size, void* d_ws, size_t ws_size, hipStream_t stream) {
  const u16* x = (const u16*)d_in[0]; const u16* ws_re = (const u16*)d_in[1]; const u16* ws_im = (const u16*)d_in[2];
  const u16* wd_re = (const u16*)d_in[3]; const u16* wd_im = (const u16*)d_in[4];
  const u16* w1 = (const u16*)d_in[6]; const u16* b1 = (const u16*)d_in[7]; const u16* w2 = (const u16*)d_in[8]; const u16* b2 = (const u16*)d_in[9];
  unsigned char* ws = (unsigned char*)d_ws;
  (void)hipFuncSetAttribute((const void*)k_main, hipFuncAttributeMaxDynamicSharedMemorySize, LDSM);
  k_ln<<<256, 512, 0, stream>>>(x, wd_re, wd_im, w1, b1, b2, ws);
  k_main<<<256, 256, LDSM, stream>>>(x, ws_re, ws_im, wd_re, wd_im, w1, b1, w2, b2, ws, (u16*)d_out);
}
```

```cpp
#include <hip/hip_runtime.h>
#include <hip/hip_bf16.h>
#include <type_traits>
#include <utility>

#define LAS __attribute__((address_space(3)))
#define DI __device__ __forceinline__
typedef unsigned short u16;
typedef short bf16x4 __attribute__((ext_vector_type(4)));
typedef float f32x4 __attribute__((ext_vector_type(4)));
typedef float f32x2 __attribute__((ext_vector_type(2)));
typedef unsigned u32x4 __attribute__((ext_vector_type(4)));
typedef unsigned u32x2 __attribute__((ext_vector_type(2)));

constexpr int S_ = 2048, D_ = 1024, F_ = 3584;
constexpr size_t WS_FLAG = 0;
constexpr int FL_E = 0, FL_X = 64, FL_AB = FL_X + 256, FL_H = FL_AB + 512, FL_W2 = FL_H + 256, FL_Y = FL_W2 + 128;
constexpr size_t WS_XA = 65536;
constexpr size_t WS_AB = WS_XA + (size_t)S_ * D_ * 2;
constexpr size_t WS_H = WS_AB + (size_t)S_ * 2 * D_ * 2;
constexpr size_t WS_W2 = WS_H + (size_t)S_ * F_ * 2;
constexpr size_t WS_PY = WS_W2 + (size_t)F_ * D_ * 2;
constexpr unsigned OOB = 0x40000000u;
constexpr int SPIN_CAP = 1 << 20;

template <int N> DI void wait_vm() { asm volatile("s_waitcnt vmcnt(%0)" ::"n"(N) : "memory"); }
DI void wait_lgkm0() { asm volatile("s_waitcnt lgkmcnt(0)" ::: "memory"); }
template <int N> DI void wait_lgkm() { asm volatile("s_waitcnt lgkmcnt(%0)" ::"n"(N > 15 ? 15 : N) : "memory"); }
DI void bar() { asm volatile("s_barrier" ::: "memory"); }
DI unsigned cvt_pk_bf16(float lo, float hi) {
  typedef __bf16 bf16x2 __attribute__((ext_vector_type(2)));
  f32x2 v = {lo, hi}; bf16x2 b = __builtin_convertvector(v, bf16x2); return __builtin_bit_cast(unsigned, b);
}
DI float bf_lo(unsigned w) { return __uint_as_float(w << 16); }
DI float bf_hi(unsigned w) { return __uint_as_float(w & 0xffff0000u); }
DI float rbf(float v) { return bf_lo(cvt_pk_bf16(v, v)); }
template <int I, int N, class F> DI void static_for(F&& f) { if constexpr (I < N) { f(std::integral_constant<int, I>{}); static_for<I + 1, N>(f); } }

#define ACLOB "a0","a1","a2","a3","a4","a5","a6","a7","a8","a9","a10","a11","a12","a13","a14","a15","a16","a17","a18","a19","a20","a21","a22","a23","a24","a25","a26","a27","a28","a29","a30","a31","a32","a33","a34","a35","a36","a37","a38","a39","a40","a41","a42","a43","a44","a45","a46","a47","a48","a49","a50","a51","a52","a53","a54","a55","a56","a57","a58","a59","a60","a61","a62","a63","a64","a65","a66","a67","a68","a69","a70","a71","a72","a73","a74","a75","a76","a77","a78","a79","a80","a81","a82","a83","a84","a85","a86","a87","a88","a89","a90","a91","a92","a93","a94","a95","a96","a97","a98","a99","a100","a101","a102","a103","a104","a105","a106","a107","a108","a109","a110","a111","a112","a113","a114","a115","a116","a117","a118","a119","a120","a121","a122","a123","a124","a125","a126","a127","a128","a129","a130","a131","a132","a133","a134","a135","a136","a137","a138","a139","a140","a141","a142","a143","a144","a145","a146","a147","a148","a149","a150","a151","a152","a153","a154","a155","a156","a157","a158","a159","a160","a161","a162","a163","a164","a165","a166","a167","a168","a169","a170","a171","a172","a173","a174","a175","a176","a177","a178","a179","a180","a181","a182","a183","a184","a185","a186","a187","a188","a189","a190","a191"
template <int RA, int RB> DI void mfma16(f32x4& acc) { asm volatile("v_mfma_f32_16x16x32_bf16 %0, a[%c1:%c2], a[%c3:%c4], %0" : "+v"(acc) : "n"(RA), "n"(RA + 3), "n"(RB), "n"(RB + 3)); }
template <int RD, int OFF> DI void ds_r128(unsigned addr) { asm volatile("ds_read_b128 a[%c1:%c2], %0 offset:%c3" ::"v"(addr), "n"(RD), "n"(RD + 3), "n"(OFF) : "memory"); }
template <int RD, int OFF> DI void ds_rtr(unsigned addr) { asm volatile("ds_read_b64_tr_b16 a[%c1:%c2], %0 offset:%c3" ::"v"(addr), "n"(RD), "n"(RD + 1), "n"(OFF) : "memory"); }
DI void wait_frags() { asm volatile("s_waitcnt lgkmcnt(0)" ::: "memory", ACLOB); }
template <int OFF, int POL> DI void dma16(int voff, __amdgpu_buffer_rsrc_t r, unsigned soff) {
  if constexpr (POL == 0) asm volatile("buffer_load_dwordx4 %0, %1, %2 offen offset:%3 lds" ::"v"(voff), "s"(r), "s"(soff), "n"(OFF) : "memory");
  if constexpr (POL == 1) asm volatile("buffer_load_dwordx4 %0, %1, %2 offen offset:%3 sc1 lds" ::"v"(voff), "s"(r), "s"(soff), "n"(OFF) : "memory");
  if constexpr (POL == 2) asm volatile("buffer_load_dwordx4 %0, %1, %2 offen offset:%3 nt lds" ::"v"(voff), "s"(r), "s"(soff), "n"(OFF) : "memory");
}
DI void pinacc(f32x4& v) { asm volatile("" : "+v"(v)); }
template <int C> DI int s_eq(int v) { int r; asm volatile("s_cmp_eq_u32 %1, %2\n\ts_cselect_b32 %0, 1, 0" : "=s"(r) : "s"(v), "n"(C) : "scc"); return r; }
DI __amdgpu_buffer_rsrc_t mkrsrc(const void* p, unsigned bytes) { return __builtin_amdgcn_make_buffer_rsrc((void*)p, 0, bytes, 0x00020000); }

#define STAMP(k, i) do {} while (0)
#define STAGGER 0
#define SNAKE 1
#define LN_PASS0 0
enum { ROWK = 0, FRAG = 1, NMAJ = 2 };

template <int KIND, int R, int POL> struct Stream {
  static constexpr bool ODD = KIND == NMAJ && R == 224;
  static constexpr int P = R / 32, NG = (P + 3) / 4, NV = ODD ? 7 : (KIND == FRAG ? 2 : 4);
  __amdgpu_buffer_rsrc_t rs;
  int voff[NV];
  unsigned s0[NG], ks;
  unsigned ml[NG];
  static DI int gpiece(int g, int wid) { if constexpr (ODD) return wid * 7 + 4 * g; else return 16 * g + 4 * wid; }
  DI void init_rowk(const void* p, unsigned bytes, int ld, int r0, int k0, int wid, int lane) {
    rs = mkrsrc(p, bytes);
#pragma unroll
    for (int j = 0; j < 4; ++j) voff[j] = ((lane >> 3) + 8 * j) * ld * 2 + (((lane & 7) ^ ((4 * (j & 1) + (lane >> 4)) & 7)) * 16) - j * 1024;
#pragma unroll
    for (int g = 0; g < NG; ++g) s0[g] = (unsigned)((r0 + 8 * gpiece(g, wid)) * ld + k0) * 2u;
    ks = 128u;
  }
  DI void init_frag(const void* p, unsigned bytes, int kbn, int r0, int k0, int wid, int lane) {
    rs = mkrsrc(p, bytes);
    voff[0] = lane * 16; voff[1] = lane * 16 + kbn * 1024 - 2048;
#pragma unroll
    for (int g = 0; g < NG; ++g) s0[g] = (unsigned)((r0 / 16 + gpiece(g, wid) / 2) * kbn + k0 / 32) * 1024u;
    ks = 2048u;
  }
  DI void init_nmaj(const void* p, unsigned bytes, int ld, int n0, int k0, int wid, int lane) {
    rs = mkrsrc(p, bytes);
    if constexpr (ODD) {
#pragma unroll
      for (int j = 0; j < 7; ++j) { const int L = (wid * 7 + j) * 64 + lane, kk = L / 28, pos = L % 28;
        voff[j] = kk * ld * 2 + (pos ^ (((kk >> 3) & 1) << 1)) * 16 - (j & 3) * 1024; }
      s0[0] = s0[1] = (unsigned)(k0 * ld + n0) * 2u;
    } else {
      static_assert(R == 128 || ODD, "");
#pragma unroll
      for (int j = 0; j < 4; ++j) { const int kk = 4 * (4 * wid + j) + (lane >> 4), g = (kk & 3) | (((kk >> 3) & 1) << 2);
        voff[j] = (4 * j + (lane >> 4)) * ld * 2 + (((lane & 15) ^ (g << 1)) * 16) - j * 1024; }
      s0[0] = (unsigned)((k0 + 16 * wid) * ld + n0) * 2u;
    }
    ks = (unsigned)(64 * ld) * 2u;
  }
  DI void init_lds(unsigned base, int wid) {
#pragma unroll
    for (int g = 0; g < NG; ++g) ml[g] = base + (unsigned)gpiece(g, wid) * 1024u;
  }
  template <int I, int SO> DI void m0() const { asm volatile("s_add_u32 m0, %0, %1" ::"s"(ml[I / 4]), "n"(SO) : "memory", "scc"); }
  template <int I> DI void issue() const {
    constexpr int g = I / 4, j = I % 4, vi = ODD ? I : (KIND == FRAG ? (j >> 1) : j);
    dma16<j * 1024, POL>(voff[vi], rs, s0[g]);
  }
  template <int I> DI void advance() { asm volatile("s_add_u32 %0, %0, %1" : "+s"(s0[I / 4]) : "s"(ks) : "scc"); }
};

template <int KIND, int R, int T, int NS, int STAGE> struct Reader {
  static constexpr int NA = KIND == FRAG ? 1 : (KIND == ROWK ? 2 : 3);
  static constexpr int SPAN = KIND == NMAJ ? 64 * R * 2 : T * 2048, SPW = (65536 - SPAN) / STAGE + 1, NW = (NS + SPW - 1) / SPW;
  unsigned a[NW][NA];
  DI void init(unsigned base, int t0, int lane) {
    const int fr = lane & 15, fq = lane >> 4;
    if constexpr (KIND == ROWK) { a[0][0] = base + (t0 * 16 + fr) * 128 + ((fq ^ (fr >> 1)) * 16); a[0][1] = base + (t0 * 16 + fr) * 128 + (((4 + fq) ^ (fr >> 1)) * 16); }
    else if constexpr (KIND == FRAG) a[0][0] = base + t0 * 2048 + lane * 16;
    else { static_assert(KIND != NMAJ || R == 224, "");
      const int q = fr >> 2, p = lane & 3, wn = t0 / 7; const unsigned b = base + (8 * fq + q) * 448 + 8 * p;
      a[0][0] = b + 32 * ((6 * wn) ^ (fq & 1)); a[0][1] = b + 32 * ((6 * wn + 1) ^ (fq & 1)); a[0][2] = b + 32 * ((12 + wn) ^ (fq & 1)); }
#pragma unroll
    for (int w = 1; w < NW; ++w)
#pragma unroll
      for (int n = 0; n < NA; ++n) { a[w][n] = a[0][n] + w * SPW * STAGE; asm volatile("" : "+v"(a[w][n])); }
  }
  template <int ST, int KS, int I, int RB> DI void rd() const {
    constexpr int w = ST / SPW, so = (ST % SPW) * STAGE;
    if constexpr (KIND == ROWK) ds_r128<RB + 4 * I, so + I * 2048>(a[w][KS]);
    else if constexpr (KIND == FRAG) ds_r128<RB + 4 * I, so + I * 2048 + KS * 1024>(a[w][0]);
    else { constexpr int n = I / 2, h = I % 2; ds_rtr<RB + 4 * n + 2 * h, so + (32 * KS + 4 * h) * (R * 2) + (n < 6 ? 32 * (n & ~1) : 0)>(a[w][n < 6 ? (n & 1) : 2]); }
  }
};

template <int BM_, int BN_, int NS_, int KA_, int KB_, int PLA_, int PLB_, bool TR_, int NX_, bool LAZY_, int RSP_> struct Geo {
  static constexpr bool LAZY = LAZY_; static constexpr int NX = NX_, PLA = PLA_, PLB = PLB_;
  static constexpr int BM = BM_, BN = BN_, NS = NS_, KA = KA_, KB = KB_, MT = BM / 32, NT = BN / 32, NM = MT * NT;
  static constexpr bool TR = TR_;
  static constexpr int PA = BM / 32, PB = BN / 32, ND = PA + PB, STAGE = (BM + BN) * 128;
  static constexpr int DEPTH = LAZY ? NS - 1 : NS;
  static constexpr int H = (DEPTH == 2) ? ND : ND / 2;
  static constexpr int NIA = KA == NMAJ ? 2 * MT : MT, NIB = KB == NMAJ ? 2 * NT : NT, NR = NIA + NIB, RSPAN = RSP_;
  static constexpr int rgap(int r) { return (r * RSPAN) / NR; }
  static constexpr int rbefore(int i) { int c = 0; for (int r = 0; r < NR; ++r) c += rgap(r) < i; return c; }
  typedef Reader<KA, BM, MT, NS, STAGE> RA; typedef Reader<KB, BN, NT, NS, STAGE> RB;
};
struct Side { __amdgpu_buffer_rsrc_t rs; unsigned voff, step, soff; };
template <class G> struct Pipe {
  Stream<G::KA, G::BM, G::PLA> A; Stream<G::KB, G::BN, G::PLB> B;
  Side xl, x2;
  unsigned ls = 0;
  unsigned* sig = nullptr; unsigned tag = 0;
  unsigned lds; int wid;
  DI void side() {
    if constexpr (G::NX > 0) asm volatile("buffer_load_dword a124, %0, %1, %2 offen" ::"v"(xl.voff), "s"(xl.rs), "s"(xl.soff) : "memory");
    if constexpr (G::NX > 1) asm volatile("buffer_load_dword a124, %0, %1, %2 offen" ::"v"(x2.voff), "s"(x2.rs), "s"(x2.soff) : "memory"); }
  DI void side_adv() {
    if constexpr (G::NX > 0) asm volatile("s_add_u32 %0, %0, %1" : "+s"(xl.soff) : "s"(xl.step) : "scc");
    if constexpr (G::NX > 1) asm volatile("s_add_u32 %0, %0, %1" : "+s"(x2.soff) : "s"(x2.step) : "scc"); }
  static constexpr bool gstart(int i) { return i < G::PA ? (i % 4 == 0) : ((i - G::PA) % 4 == 0); }
  static constexpr bool gend(int i) { return i == G::PA - 1 || i == G::ND - 1 || gstart(i + 1); }
  template <int I, int ST> DI void m0() const { if constexpr (I < G::PA) A.template m0<I, ST * G::STAGE>(); else B.template m0<I - G::PA, ST * G::STAGE>(); }
  template <int I> DI void issue() const { if constexpr (I < G::PA) A.template issue<I>(); else B.template issue<I - G::PA>(); }
  template <int I> DI void advance() { if constexpr (I < G::PA) A.template advance<I>(); else B.template advance<I - G::PA>(); }
  template <int MASK, bool LAST> DI void prologue() {
    if constexpr (MASK & 1) A.init_lds(lds, wid);
    if constexpr (MASK & 2) B.init_lds(lds + G::BM * 128, wid);
    asm volatile("s_nop 4" ::: "memory", ACLOB);
    static_for<0, G::DEPTH>([&](auto Ss) {
      if constexpr (LAST && Ss.value >= 2) { side(); side_adv(); }
      static_for<0, (Ss.value == G::DEPTH - 1 ? G::H : G::ND)>([&](auto Dd) { constexpr int d = Dd.value;
        if constexpr ((MASK >> (d < G::PA ? 0 : 1)) & 1) {
          if constexpr (gstart(d)) { m0<d, Ss.value>(); asm volatile("s_nop 0"); }
          issue<d>();
          if constexpr (gend(d)) advance<d>(); }
        else if constexpr (LAST && Ss.value >= 1) asm volatile("buffer_load_dword a124, off, %0, %1" ::"s"(A.rs), "s"(OOB) : "memory"); }); });
    if constexpr (LAST && G::H < G::ND) if constexpr (!gstart(G::H)) m0<G::H, G::DEPTH - 1>();
  }
};

constexpr int dgap(int d, int ndp, int nm, int w) { return 1 + (d * (nm - 2) + w * (nm - 2) / 4) / ndp; }
constexpr int dat(int i, int ndp, int nm, int w) { for (int d = 0; d < ndp; ++d) if (dgap(d, ndp, nm, w) == i) return d; return -1; }
template <class G, int W, int CUR, int RST, int RKS, int D0, int D1, int DST, int I>
DI void gap(Pipe<G>& p, const typename G::RA& ra, const typename G::RB& rb, f32x4 (&acc)[G::MT][G::NT]) {
  constexpr int CA = 64 * CUR, CB = CA + 32, NA = 64 * (CUR ^ 1), NB = NA + 32;
  constexpr int NM = G::NM, NIB = G::NIB, NR = G::NR, NDP = D1 - D0, m = I / G::NT, n0 = I % G::NT, n = (SNAKE && (m & 1)) ? G::NT - 1 - n0 : n0;
  constexpr int d = dat(I, NDP, NM, W), dp = dat(I - 1, NDP, NM, W);
  if constexpr (n0 == 0 && m % 2 == 0 && (G::LAZY || CUR == 0)) wait_lgkm<(NR - 1 - (NIB + m + 1)) + G::rbefore(I)>();
  if constexpr (d >= 0) if constexpr (Pipe<G>::gstart(D0 + d)) p.template m0<D0 + d, DST>();
  if constexpr (G::TR) mfma16<CB + 4 * n, CA + 4 * m>(acc[m][n]); else mfma16<CA + 4 * m, CB + 4 * n>(acc[m][n]);
  if constexpr (I == 0 && CUR == (G::DEPTH == 2 ? 0 : 1)) p.side();
  if constexpr (I == NM - 1 && CUR == (G::DEPTH == 2 ? 0 : 1)) p.side_adv();
  if constexpr (dp >= 0) if constexpr (Pipe<G>::gend(D0 + dp)) p.template advance<D0 + dp>();
  if constexpr (d >= 0) p.template issue<D0 + d>();
  static_for<G::rbefore(I), G::rbefore(I + 1)>([&](auto Rr) { constexpr int r = Rr.value;
    if constexpr (r == 0) ra.template rd<RST, RKS, 0, NA>();
    else if constexpr (r <= NIB) rb.template rd<RST, RKS, r - 1, NB>();
    else ra.template rd<RST, RKS, r - NIB, NA>(); });
}
template <class G, int W, int CUR, int RST, int RKS, int D0, int D1, int DST, int... Is>
DI void phase_(Pipe<G>& p, const typename G::RA& ra, const typename G::RB& rb, f32x4 (&acc)[G::MT][G::NT], std::integer_sequence<int, Is...>) {
  (gap<G, W, CUR, RST, RKS, D0, D1, DST, Is>(p, ra, rb, acc), ...);
}
template <class G, int W, int ST>
DI void ktile(Pipe<G>& p, const typename G::RA& ra, const typename G::RB& rb, f32x4 (&acc)[G::MT][G::NT]) {
  constexpr int NS = G::NS, ND = G::ND, H = G::H, DP = G::DEPTH;
  phase_<G, W, 0, ST, 1, H, ND, (ST + DP - 1) % NS>(p, ra, rb, acc, std::make_integer_sequence<int, G::NM>{});
  if constexpr (G::LAZY) asm volatile("s_waitcnt vmcnt(%0)\n\ts_barrier" ::"n"((DP - 2) * (ND + G::NX)) : "memory", ACLOB);
  else asm volatile("s_waitcnt vmcnt(%0) lgkmcnt(0)\n\ts_barrier" ::"n"(DP == 2 ? G::NX : (DP - 2) * (ND + G::NX)) : "memory", ACLOB);
  phase_<G, W, 1, (ST + 1) % NS, 0, 0, H, (ST + DP) % NS>(p, ra, rb, acc, std::make_integer_sequence<int, G::NM>{});
}
template <class G, int W, int NTILE, class Hook>
DI void gemm_body(Pipe<G>& p, const typename G::RA& ra, const typename G::RB& rb, f32x4 (&acc)[G::MT][G::NT], int on, Hook hook) {
#pragma nounroll
  for (int it = on * (NTILE / G::NS); it > 0; --it) { static_for<0, G::NS>([&](auto Ss) { ktile<G, W, Ss.value>(p, ra, rb, acc); });
    hook(it);
  }
  if constexpr (NTILE % G::NS != 0) {
    asm volatile("" : "+s"(on));
#pragma nounroll
    for (int it = on; it > 0; --it) static_for<0, NTILE % G::NS>([&](auto Ss) { ktile<G, W, Ss.value>(p, ra, rb, acc); });
  }
}
template <class G, int NTILE, class Init, class Hook>
DI void gemm_loop(Pipe<G>& p, int lane, f32x4 (&acc)[G::MT][G::NT], int st0, Init init, Hook hook) {
  constexpr int ND = G::ND, H = G::H, DP = G::DEPTH;
  typename G::RA ra; typename G::RB rb;
  ra.init(p.lds, (p.wid >> 1) * G::MT, lane); rb.init(p.lds + G::BM * 128, (p.wid & 1) * G::NT, lane);
#pragma unroll
  for (int n = 0; n < G::NT; ++n) { const f32x4 v = init(n);
#pragma unroll
    for (int m = 0; m < G::MT; ++m) { acc[m][n] = v; pinacc(acc[m][n]); } }
  wait_vm<(DP - 2) * (ND + G::NX) + H>();
  bar();
  if (p.sig != nullptr && threadIdx.x == 0) __hip_atomic_store(p.sig, p.tag, __ATOMIC_RELAXED, __HIP_MEMORY_SCOPE_AGENT);
  static_for<0, G::NIA>([&](auto I) { ra.template rd<0, 0, I.value, 0>(); });
  static_for<0, G::NIB>([&](auto I) { rb.template rd<0, 0, I.value, 32>(); });
  wait_frags();
  STAMP(1, st0);
  asm volatile("s_nop 1");
  gemm_body<G, 0, NTILE>(p, ra, rb, acc, 1, hook);
  asm volatile("s_nop 7\n\ts_nop 7\n\ts_waitcnt vmcnt(0) lgkmcnt(0)" ::: "memory", ACLOB);
#pragma unroll
  for (int m = 0; m < G::MT; ++m)
#pragma unroll
    for (int n = 0; n < G::NT; ++n) pinacc(acc[m][n]);
  STAMP(1, st0 + 1);
}

DI void touch(const void* p) { asm volatile("global_load_dword a124, %0, off" ::"v"(p) : "memory"); }
template <int R, int OFF> DI void gld4_nt(const void* p) { asm volatile("global_load_dwordx4 a[%c1:%c2], %0, off offset:%c3 nt" ::"v"(p), "n"(R), "n"(R + 3), "n"(OFF) : "memory"); }
template <int R> DI void gld2(const void* p) { asm volatile("global_load_dwordx2 a[%c1:%c2], %0, off" ::"v"(p), "n"(R), "n"(R + 1) : "memory"); }
template <int R, int OFF> DI void bld4_dev(__amdgpu_buffer_rsrc_t r, unsigned voff, unsigned soff) { asm volatile("buffer_load_dwordx4 a[%c3:%c4], %0, %1, %2 offen offset:%c5 sc1" ::"v"(voff), "s"(r), "s"(soff), "n"(R), "n"(R + 3), "n"(OFF) : "memory"); }
template <int R> DI void flag_req(const unsigned* p) { asm volatile("global_load_dword a[%c1], %0, off sc1" ::"v"(p), "n"(R) : "memory"); }
template <int R> DI unsigned ard() { unsigned r; asm volatile("v_accvgpr_read_b32 %0, a[%c1]" : "=v"(r) : "n"(R)); return r; }
template <int R> DI u32x4 ard4() { return (u32x4){ard<R>(), ard<R + 1>(), ard<R + 2>(), ard<R + 3>()}; }
template <int R> DI u32x2 ard2() { return (u32x2){ard<R>(), ard<R + 1>()}; }
template <int CTRL> DI float dpp_add(float a) { return a + __builtin_bit_cast(float, __builtin_amdgcn_update_dpp(0, __builtin_bit_cast(int, a), CTRL, 0xf, 0xf, true)); }
template <int N> DI void wave_sum(float (&s)[N]) {
#pragma unroll
  for (int r = 0; r < N; ++r) s[r] = dpp_add<0xB1>(s[r]);
#pragma unroll
  for (int r = 0; r < N; ++r) s[r] = dpp_add<0x4E>(s[r]);
#pragma unroll
  for (int r = 0; r < N; ++r) s[r] = dpp_add<0x141>(s[r]);
#pragma unroll
  for (int r = 0; r < N; ++r) s[r] = dpp_add<0x140>(s[r]);
#pragma unroll
  for (int r = 0; r < N; ++r) { const auto p = __builtin_amdgcn_permlane16_swap(__float_as_uint(s[r]), __float_as_uint(s[r]), false, false); s[r] = __uint_as_float(p[0]) + __uint_as_float(p[1]); }
#pragma unroll
  for (int r = 0; r < N; ++r) { const auto p = __builtin_amdgcn_permlane32_swap(__float_as_uint(s[r]), __float_as_uint(s[r]), false, false); s[r] = __uint_as_float(p[0]) + __uint_as_float(p[1]); }
}

DI f32x2 sigm_mul(f32x2 x, f32x2 arg) {
  f32x2 e; e.x = __builtin_amdgcn_exp2f(arg.x); e.y = __builtin_amdgcn_exp2f(arg.y);
  const f32x2 d = e + 1.0f;
  f32x2 r; r.x = __builtin_amdgcn_rcpf(d.x); r.y = __builtin_amdgcn_rcpf(d.y);
  return x * r;
}
DI f32x2 gelu2(f32x2 x) {
  const float k0 = -2.0f * 0.7978845608028654f * 1.4426950408889634f, k1 = k0 * 0.044715f;
  return sigm_mul(x, x * ((x * x) * k1 + k0));
}
DI f32x2 silu2(f32x2 x) { return sigm_mul(x, x * -1.4426950408889634f); }
DI f32x4 gelu4(f32x4 t) { const f32x2 a = gelu2((f32x2){t[0], t[1]}), b = gelu2((f32x2){t[2], t[3]}); return (f32x4){a.x, a.y, b.x, b.y}; }
DI f32x4 bf4(u32x2 w) { return (f32x4){bf_lo(w[0]), bf_hi(w[0]), bf_lo(w[1]), bf_hi(w[1])}; }

#define GO_NS 4
#define GO_LAZY false
#define GO_RSP 11
#define DEVPOL 1
typedef Geo<256, 128, 3, FRAG, ROWK, DEVPOL, 0, false, 1, false, 24> GAB;
#define YT1 3
#define YT2 11
#define ABT 4
#define PREP_DELAY 16
#define ABS 8
#define YS0 5
#define YSN 5
typedef Geo<256, 128, 3, ROWK, FRAG, 0, DEVPOL, true, 1, false, 24> GY;
#define H_TOUCH (ABT < 4)
typedef Geo<256, 224, 2, FRAG, NMAJ, DEVPOL, 0, true, 0, false, 42> GH;
typedef Geo<128, 128, GO_NS, FRAG, FRAG, DEVPOL, DEVPOL, true, 0, GO_LAZY, GO_RSP> GO;
constexpr int LDSM = GY::NS * GY::STAGE > GO::NS * GO::STAGE ? GY::NS * GY::STAGE : GO::NS * GO::STAGE;

DI u32x4 pack_pair(const f32x4& a, const f32x4& b) {
  const unsigned a0 = cvt_pk_bf16(a[0], a[1]), a1 = cvt_pk_bf16(a[2], a[3]), b0 = cvt_pk_bf16(b[0], b[1]), b1 = cvt_pk_bf16(b[2], b[3]);
  const auto r0 = __builtin_amdgcn_permlane16_swap(a0, b0, false, false);
  const auto r1 = __builtin_amdgcn_permlane16_swap(a1, b1, false, false);
  return (u32x4){r0[0], r1[0], r0[1], r1[1]};
}
DI u32x4 pack8(const f32x4& a, const f32x4& b) { return (u32x4){cvt_pk_bf16(a[0], a[1]), cvt_pk_bf16(a[2], a[3]), cvt_pk_bf16(b[0], b[1]), cvt_pk_bf16(b[2], b[3])}; }
DI unsigned hpos(int t, int b) { return b == 0 ? 2 * t : (b == 3 ? 2 * t + 1 : 32 + 5 * t + (b < 3 ? b - 1 : b - 2)); }
struct NoHook { DI void operator()(int) const {} };
struct ZeroInit { DI f32x4 operator()(int) const { return (f32x4){0.f, 0.f, 0.f, 0.f}; } };

DI void st_dev(__amdgpu_buffer_rsrc_t r, unsigned off, u32x4 v) { __builtin_amdgcn_raw_buffer_store_b128(v, r, off, 0, 16); }
DI void st_dev8(__amdgpu_buffer_rsrc_t r, unsigned off, u32x2 v) { __builtin_amdgcn_raw_buffer_store_b64(v, r, off, 0, 16); }
DI u32x4 ld_dev(__amdgpu_buffer_rsrc_t r, unsigned voff, unsigned soff) { return __builtin_amdgcn_raw_buffer_load_b128(r, voff, soff, 16); }
DI void signal(unsigned* f, unsigned tag) { if (threadIdx.x == 0) __hip_atomic_store(f, tag, __ATOMIC_RELAXED, __HIP_MEMORY_SCOPE_AGENT); }
DI void publish(unsigned* f, unsigned tag) { wait_vm<0>(); bar(); signal(f, tag); }
DI void poll(unsigned* f, int n, unsigned tag) {
  unsigned* q = f + ((int)(threadIdx.x & 63) < n ? (threadIdx.x & 63) : 0);
#pragma nounroll
  for (int i = 0; i < SPIN_CAP && !__all(__hip_atomic_load(q, __ATOMIC_RELAXED, __HIP_MEMORY_SCOPE_AGENT) == tag); ++i) __builtin_amdgcn_s_sleep(1);
}
DI void await(unsigned* f, int n, unsigned tag) {
  if (threadIdx.x < 64) poll(f, n, tag);
  asm volatile("" ::: "memory");
  bar();
}

__global__ __launch_bounds__(256, 1) void k_main(const u16* __restrict__ x, const u16* __restrict__ ws_re, const u16* __restrict__ ws_im, const u16* __restrict__ wd_re, const u16* __restrict__ wd_im,
                                                 const u16* __restrict__ w1, const u16* __restrict__ b1, const u16* __restrict__ w2, const u16* __restrict__ b2, unsigned char* ws, u16* __restrict__ out) {
  extern __shared__ __attribute__((aligned(16))) unsigned char shm[];
  {
    const char* pc; unsigned len;
    asm volatile("s_getpc_b64 %0\n.Lkm_pc:\n\ts_mov_b32 %1, .Lkm_endy-.Lkm_pc\n\ts_max_i32 %1, %1, .Lkm_endo-.Lkm_pc" : "=s"(pc), "=s"(len) :: "scc");
    asm volatile("buffer_load_dword a124, %0, %1, 0 offen" ::"v"((((blockIdx.x >> 3) & 1) * 256 + threadIdx.x) * 128), "s"(mkrsrc(pc, len)) : "memory", ACLOB);
  }
  const int wid = __builtin_amdgcn_readfirstlane(threadIdx.x >> 6), lane = threadIdx.x & 63, fr = lane & 15, fq = lane >> 4, wm = wid >> 1, wn = wid & 1;
  const int xcd = blockIdx.x & 7, slot = blockIdx.x >> 3, chain = slot >> 4, s4 = slot & 15;
  const int pm1 = (xcd & 1) * 4 + (s4 & 3), pn1 = (xcd >> 1) * 4 + (s4 >> 2);
  const int half = xcd & 1, pm = (xcd >> 2) * 4 + (s4 & 3), pn = ((xcd >> 1) & 1) * 4 + (s4 >> 2), tile = pm * 8 + pn;
  const unsigned ldsb = (unsigned)(size_t)(LAS unsigned char*)shm;
  const int lo = ((fq & 1) * 2 + (fq >> 1)) * 256 + fr * 16;
  unsigned* flags = (unsigned*)(ws + WS_FLAG);
  const __amdgpu_buffer_rsrc_t ry = mkrsrc(ws + WS_PY + (size_t)tile * 131072, 131072);
  STAMP(1, 0);
  const int kb = xcd * 16 + s4;
  unsigned tag;
  {
    const u16* px = x + (size_t)(blockIdx.x * 8 + wid * 2) * D_ + lane * 8;
    asm volatile("global_load_dword a16, %0, off sc1" ::"v"(flags + FL_E) : "memory");
    gld4_nt<0, 0>(px); gld4_nt<4, 1024>(px); gld4_nt<8, 2048>(px); gld4_nt<12, 3072>(px); }
  auto layernorm = [&](auto NY) {
    wait_vm<NY.value>();
    const u32x4 raw[4] = {ard4<0>(), ard4<4>(), ard4<8>(), ard4<12>()};
    const unsigned ev = ard<16>();
    f32x2 v[2][8]; float sm[2], mean[2];
#pragma unroll
    for (int i = 0; i < 16; ++i) v[i >> 3][i & 7] = (f32x2){bf_lo(raw[i >> 2][i & 3]), bf_hi(raw[i >> 2][i & 3])};
#pragma unroll
    for (int r = 0; r < 2; ++r) { const f32x2 a = ((v[r][0] + v[r][1]) + (v[r][2] + v[r][3])) + ((v[r][4] + v[r][5]) + (v[r][6] + v[r][7])); sm[r] = a.x + a.y; }
    wave_sum(sm);
#pragma unroll
    for (int r = 0; r < 2; ++r) { mean[r] = sm[r] * (1.0f / D_); f32x2 a = {0.f, 0.f};
#pragma unroll
      for (int e = 0; e < 8; ++e) { const f32x2 dd = v[r][e] - mean[r]; a += dd * dd; }
      sm[r] = a.x + a.y; }
    wave_sum(sm);
    LAS unsigned char* tl = (LAS unsigned char*)shm;
#pragma unroll
    for (int r = 0; r < 2; ++r) { const float rden = 1.0f / rbf(sqrtf(rbf(rbf(sm[r] * (1.0f / D_)) + 1e-5f))), mb = rbf(mean[r]);
#pragma unroll
      for (int j = 0; j < 2; ++j) { u32x4 o;
#pragma unroll
        for (int e = 0; e < 4; ++e) { const f32x2 d = v[r][4 * j + e] - mb; const unsigned db = cvt_pk_bf16(d.x, d.y); const f32x2 q = (f32x2){bf_lo(db), bf_hi(db)} * rden; o[e] = cvt_pk_bf16(q.x, q.y); }
        *(LAS u32x4*)(tl + (j * 64 + lane) * 128 + ((wid * 2 + r + lane) & 7) * 16) = o; } }
    tag = __builtin_amdgcn_readfirstlane(ev) + 1u;
    wait_lgkm0(); bar();
    STAMP(1, 14);
    const __amdgpu_buffer_rsrc_t rx = mkrsrc(ws + WS_XA + (size_t)(blockIdx.x >> 1) * 32768, 32768);
#pragma unroll
    for (int j = 0; j < 4; ++j) { const int ch = (wid * 4 + j) * 8 + (lane >> 3);
      st_dev(rx, ch * 256 + (blockIdx.x & 1) * 128 + (lane & 7) * 16, *(const LAS u32x4*)(tl + ch * 128 + ((lane + ch) & 7) * 16)); }
    publish(flags + FL_X + blockIdx.x, tag);
    STAMP(1, 10);
  };
  if (chain == 0) {
    const u16* wsm = half ? ws_im : ws_re;
    const u16* wshare = wsm + (size_t)(pm * 256 + pn * 32) * S_;
    f32x4 acc[8][4];
    {
      Pipe<GAB> p; p.lds = ldsb; p.wid = wid;
      p.B.init_rowk(pn1 < 8 ? wd_re : wd_im, D_ * D_ * 2, D_, (pn1 & 7) * 128, 0, wid, lane);
      p.prologue<2, false>();
      layernorm(std::integral_constant<int, 8>{});
      __builtin_amdgcn_s_sleep(PREP_DELAY);
      {
        const int t = (s4 * 8 + xcd) * 256 + threadIdx.x;
        const int c = (s4 & 3) * 256 + threadIdx.x;
        touch((const char*)w1 + (size_t)t * 128);
        touch(t < 24576 ? (const char*)w1 + (size_t)(t + 32768) * 128 : (c < 512 ? (const char*)b1 + (c % 56) * 128 : (const char*)b2 + (c & 15) * 128));
        touch((const char*)(t < 16384 ? wd_re : wd_im) + (size_t)(t & 16383) * 128);
        const __amdgpu_buffer_rsrc_t rs = mkrsrc(w2 + (size_t)(kb < 112 ? kb : 0) * 32 * D_, kb < 112 ? 32 * D_ * 2 : 0);
        asm volatile("s_nop 4" ::: "memory");
        static_for<0, 16>([&](auto I) { constexpr int i = I.value;
          const int g = ((i >> 1) & 3) | ((wid & 1) << 2);
          asm volatile("s_add_u32 m0, %0, %1\n\ts_nop 0" ::"s"(ldsb + (1 + wm) * GAB::STAGE + wn * 16384), "n"(i * 1024) : "memory", "scc");
          dma16<0, 2>((lane ^ (g << 1)) * 16, rs, wid * 16384 + i * 1024); });
      }
      wait_vm<0>(); bar();
      if (kb < 112) {
        const int q = fr >> 2, gk = q | ((fq & 1) << 2);
        const __amdgpu_buffer_rsrc_t rw = mkrsrc(ws + WS_W2, F_ * D_ * 2);
        LAS unsigned char* src = (LAS unsigned char*)shm + (1 + (fq >> 1)) * GAB::STAGE + (8 * (fq & 1) + q) * 2048 + wid * 512 + 8 * (lane & 3);
        const unsigned kp = hpos(kb / 7, kb % 7);
#pragma unroll
        for (int n = 0; n < 16; ++n) {
          const bf16x4 l4 = __builtin_amdgcn_ds_read_tr16_b64_v4i16((LAS bf16x4*)(src + 32 * (n ^ gk)));
          const bf16x4 h4 = __builtin_amdgcn_ds_read_tr16_b64_v4i16((LAS bf16x4*)(src + 32 * (n ^ gk) + 8192));
          const u32x2 a = __builtin_bit_cast(u32x2, l4), b = __builtin_bit_cast(u32x2, h4);
          st_dev(rw, (unsigned)((wid * 16 + n) * 112 + kp) * 1024u + lane * 16, (u32x4){a[0], a[1], b[0], b[1]});
        }
        wait_lgkm0();
      }
      STAMP(1, 4);
      p.xl.rs = mkrsrc(wshare + (size_t)wid * 8 * S_, 32768); p.xl.step = 16384; p.xl.soff = 0u - (unsigned)(kb % ABS) * 16384u; p.xl.voff = fq * 4096 + fr * 128;
      p.A.init_frag(ws + WS_XA, S_ * D_ * 2, 32, pm1 * 256, 0, wid, lane);
      await(flags + FL_X + pm1 * 32, 32, tag);
      p.prologue<1, true>();
      p.sig = flags + FL_W2 + kb; p.tag = tag;
      gemm_loop<GAB, 16>(p, lane, acc, 1, ZeroInit(), NoHook());
      const __amdgpu_buffer_rsrc_t ro = mkrsrc(ws + WS_AB, S_ * 2 * D_ * 2);
      const unsigned ob = (unsigned)((pn1 * 8 + wn * 4) * 64 + pm1 * 8 + wm * 4) * 1024u + lo;
#pragma unroll
      for (int n = 0; n < 4; ++n)
#pragma unroll
        for (int m = 0; m < 8; m += 2) st_dev(ro, ob + (n * 64 + m / 2) * 1024, pack_pair(acc[m][n], acc[m + 1][n]));
      publish(flags + FL_AB + pn1 * 32 + pm1, tag);
      STAMP(1, 3);
    }
    {
      Pipe<GY> p; p.lds = ldsb; p.wid = wid;
      p.xl.rs = mkrsrc(wshare + (size_t)wid * 8 * S_, 32768); p.xl.step = 16384; p.xl.soff = 0u - (unsigned)(YS0 + kb % YSN) * 16384u; p.xl.voff = fq * 4096 + fr * 128 + 2048;
      p.A.init_rowk(wsm, S_ * S_ * 2, S_, pm * 256, 0, wid, lane);
      p.B.init_frag(ws + WS_AB, S_ * 2 * D_ * 2, 64, half * D_ + pn * 128, 0, wid, lane);
      p.prologue<1, false>();
      await(flags + FL_AB + (half * 8 + pn) * 32, 8, tag);
      STAMP(1, 5);
      p.prologue<2, true>();
      gemm_loop<GY, 32>(p, lane, acc, 6, ZeroInit(), NoHook());
#pragma unroll
      for (int i = 0; i < 16; ++i) st_dev(ry, lane * 16 + half * 65536 + wm * 32768 + (((i >> 3) * 2 + wn) * 8 + (i & 7)) * 1024, pack8(acc[i >> 1][(i & 1) * 2], acc[i >> 1][(i & 1) * 2 + 1]));
      asm volatile(".Lkm_endy:" ::: "memory");
      publish(flags + FL_Y + tile * 32 + half, tag);
      STAMP(1, 8);
    }
  } else {
    Pipe<GO> po; po.lds = ldsb; po.wid = wid;
    const size_t xo = (size_t)(pm * 256 + half * 128 + wm * 64 + (lane >> 3)) * D_ + pn * 128 + wn * 64 + (lane & 7) * 8;
    {
      const int c0 = pn1 * 224;
      Pipe<GH> p; p.lds = ldsb; p.wid = wid;
      static_for<0, 7>([&](auto N) { constexpr int n = N.value; gld2<32 + 2 * n>(b1 + c0 + (n < 6 ? wn * 96 + n * 16 : 192 + wn * 16) + fq * 4); });
      p.B.init_nmaj(w1, D_ * F_ * 2, F_, c0, 0, wid, lane);
      p.prologue<2, false>();
      layernorm(std::integral_constant<int, 21>{});
      p.A.init_frag(ws + WS_XA, S_ * D_ * 2, 32, pm1 * 256, 0, wid, lane);
      await(flags + FL_X + pm1 * 32, 32, tag);
      STAMP(1, 11);
      p.prologue<1, true>();
      f32x4 acc[8][7];
      const u32x2 bias[7] = {ard2<32>(), ard2<34>(), ard2<36>(), ard2<38>(), ard2<40>(), ard2<42>(), ard2<44>()};
      gemm_loop<GH, 16>(p, lane, acc, 1, [&](int n) { return bf4(bias[n]); }, NoHook());
      flag_req<125>(flags + FL_W2 + lane); flag_req<126>(flags + FL_W2 + 64 + lane);
      const __amdgpu_buffer_rsrc_t ro = mkrsrc(ws + WS_H, S_ * F_ * 2);
      const unsigned ob = (unsigned)((pm1 * 16 + wm * 8) * 112) * 1024u, o1 = ob + (2 * pn1 + wn) * 1024 + lo, o2 = ob + (32 + 5 * pn1 + 2 * wn) * 1024 + lo,
                     os = ob + (32 + 5 * pn1 + 4) * 1024 + (wn * 2 + (fq >> 1)) * 256 + fr * 16 + (fq & 1) * 8;
#pragma unroll
      for (int m = 0; m < 8; ++m) st_dev(ro, o1 + m * 112 * 1024, pack_pair(gelu4(acc[m][0]), gelu4(acc[m][1])));
      asm volatile("" ::: "memory");
#pragma unroll
      for (int m = 0; m < 8; ++m) {
        st_dev(ro, o2 + m * 112 * 1024, pack_pair(gelu4(acc[m][2]), gelu4(acc[m][3])));
        st_dev(ro, o2 + m * 112 * 1024 + 1024, pack_pair(gelu4(acc[m][4]), gelu4(acc[m][5])));
        const f32x4 g = gelu4(acc[m][6]); st_dev8(ro, os + m * 112 * 1024, (u32x2){cvt_pk_bf16(g[0], g[1]), cvt_pk_bf16(g[2], g[3])});
        if (m == 2) {
          wait_vm<9>(); bar(); signal(flags + FL_H + pm1 * 32 + pn1, tag);
          if (!__all(ard<125>() == tag && ard<126>() == tag)) { poll(flags + FL_W2, 64, tag); poll(flags + FL_W2 + 64, 64, tag); }
          po.B.init_frag(ws + WS_W2, F_ * D_ * 2, 112, pn * 128, 0, wid, lane);
          po.prologue<2, false>();
        }
        if (m == 6) flag_req<125>(flags + FL_H + pm * 32 + (lane & 15));
      }
      STAMP(1, 3);
    }
    f32x4 acc[4][4];
    po.A.init_frag(ws + WS_H, S_ * F_ * 2, 112, pm * 256 + half * 128, 0, wid, lane);
    wait_vm<3>();
    if (!__all(ard<125>() == tag)) poll(flags + FL_H + pm * 32, 16, tag);
    asm volatile("" ::: "memory");
    STAMP(1, 5);
    po.prologue<1, true>();
    po.sig = flags + FL_H + pm1 * 32 + 16 + pn1; po.tag = tag;
    unsigned* fh2 = flags + FL_H + pm * 32 + 16;
    unsigned* fy = flags + FL_Y + tile * 32;
    auto yreq = [&]() {
      static_for<0, 16>([&](auto I) { constexpr int i = I.value;
        bld4_dev<128 + 4 * i, (i & 3) * 1024>(ry, lane * 16, (i >> 3) * 65536 + half * 32768 + wid * 8192 + (i & 4) * 1024); });
    };
    gemm_loop<GO, 56>(po, lane, acc, 6, ZeroInit(), [&](int it) {
      if (it == 13) flag_req<125>(fh2 + (lane & 15));
      if (it == 12) { if (!__all(ard<125>() == tag)) poll(fh2, 16, tag); asm volatile("" ::: "memory"); }
      if (it == 2) { flag_req<125>(fy + (lane & 1)); static_for<0, 4>([&](auto N) { gld2<80 + 2 * N.value>(b2 + pn * 128 + wn * 64 + N.value * 16 + fq * 4); }); }
    });
    static_for<0, 8>([&](auto J) { constexpr int j = J.value; gld4_nt<16 + 4 * (j & 3) + 32 * (j >> 2), 0>(x + xo + (size_t)j * 8 * D_); });
    if (!__all(ard<125>() == tag)) poll(fy, 2, tag);
    asm volatile("" ::: "memory");
    yreq();
    STAMP(1, 8);
    const u32x2 bias[4] = {ard2<80>(), ard2<82>(), ard2<84>(), ard2<86>()};
#pragma unroll
    for (int i = 0; i < 16; ++i) { const f32x4 t = acc[i >> 2][i & 3] + bf4(bias[i & 3]);
      const f32x2 a = silu2((f32x2){t[0], t[1]}), b = silu2((f32x2){t[2], t[3]}); acc[i >> 2][i & 3] = (f32x4){a.x, a.y, b.x, b.y}; pinacc(acc[i >> 2][i & 3]); }
    wait_vm<0>(); bar();
    const u32x4 xr[8] = {ard4<16>(), ard4<20>(), ard4<24>(), ard4<28>(), ard4<48>(), ard4<52>(), ard4<56>(), ard4<60>()};
    u32x4 yr[8], yi[8];
    static_for<0, 8>([&](auto I) { yr[I.value] = ard4<128 + 4 * I.value>(); yi[I.value] = ard4<160 + 4 * I.value>(); });
    LAS unsigned char* tl = (LAS unsigned char*)shm + wid * (64 * 144);
#pragma unroll
    for (int i = 0; i < 8; ++i) {
      unsigned r[4];
#pragma unroll
      for (int e = 0; e < 4; ++e) { const f32x4 g = acc[i >> 1][(i & 1) * 2 + (e >> 1)];
        r[e] = cvt_pk_bf16((bf_lo(yr[i][e]) - bf_lo(yi[i][e])) * g[(e & 1) * 2], (bf_hi(yr[i][e]) - bf_hi(yi[i][e])) * g[(e & 1) * 2 + 1]); }
      LAS unsigned char* q = tl + ((i >> 1) * 16 + fr) * 144 + (i & 1) * 64 + fq * 8;
      *(LAS u32x2*)q = (u32x2){r[0], r[1]}; *(LAS u32x2*)(q + 32) = (u32x2){r[2], r[3]};
    }
    wait_lgkm0();
#pragma unroll
    for (int j = 0; j < 8; ++j) {
      const u32x4 r = *(const LAS u32x4*)(tl + (j * 8 + (lane >> 3)) * 144 + (lane & 7) * 16);
      u32x4 o;
#pragma unroll
      for (int e = 0; e < 4; ++e) o[e] = cvt_pk_bf16(bf_lo(xr[j][e]) + bf_lo(r[e]), bf_hi(xr[j][e]) + bf_hi(r[e]));
      __builtin_nontemporal_store(o, (u32x4*)(out + xo + (size_t)j * 8 * D_));
    }
    asm volatile(".Lkm_endo:" ::: "memory");
    if (blockIdx.x == 255) {
      poll(flags + FL_X + wid * 64, 64, tag);
      asm volatile("" ::: "memory"); bar();
      signal(flags + FL_E, tag);
    }
    STAMP(1, 9);
  }
}

extern "C" void kernel_launch(void* const* d_in, const int* in_sizes, int n_in, void* d_out, int out_size, void* d_ws, size_t ws_size, hipStream_t stream) {
  const u16* x = (const u16*)d_in[0]; const u16* ws_re = (const u16*)d_in[1]; const u16* ws_im = (const u16*)d_in[2];
  const u16* wd_re = (const u16*)d_in[3]; const u16* wd_im = (const u16*)d_in[4];
  const u16* w1 = (const u16*)d_in[6]; const u16* b1 = (const u16*)d_in[7]; const u16* w2 = (const u16*)d_in[8]; const u16* b2 = (const u16*)d_in[9];
  unsigned char* ws = (unsigned char*)d_ws;
  (void)hipFuncSetAttribute((const void*)k_main, hipFuncAttributeMaxDynamicSharedMemorySize, LDSM);
  k_main<<<256, 256, LDSM, stream>>>(x, ws_re, ws_im, wd_re, wd_im, w1, b1, w2, b2, ws, (u16*)d_out);
}
```

```cpp
#include <hip/hip_runtime.h>
#include <hip/hip_bf16.h>
#include <type_traits>
#include <utility>

#define LAS __attribute__((address_space(3)))
#define DI __device__ __forceinline__
typedef unsigned short u16;
typedef short bf16x4 __attribute__((ext_vector_type(4)));
typedef float f32x4 __attribute__((ext_vector_type(4)));
typedef float f32x2 __attribute__((ext_vector_type(2)));
typedef unsigned u32x4 __attribute__((ext_vector_type(4)));
typedef unsigned u32x2 __attribute__((ext_vector_type(2)));

constexpr int S_ = 2048, D_ = 1024, F_ = 3584;
constexpr size_t WS_FLAG = 0;
#define FS_ 16
constexpr int FS = FS_, FL_E = 0, FL_X = 32, FL_AB = FL_X + 256, FL_H = FL_AB + 128, FL_W2 = FL_H + 256, FL_Y = FL_W2 + 128;
constexpr size_t WS_XA = 262144;
constexpr size_t WS_AB = WS_XA + (size_t)S_ * D_ * 2;
constexpr size_t WS_H = WS_AB + (size_t)S_ * 2 * D_ * 2;
constexpr size_t WS_W2 = WS_H + (size_t)S_ * F_ * 2;
constexpr size_t WS_PY = WS_W2 + (size_t)F_ * D_ * 2;
constexpr unsigned OOB = 0x40000000u;
constexpr int SPIN_CAP = 1 << 20;

template <int N> DI void wait_vm() { asm volatile("s_waitcnt vmcnt(%0)" ::"n"(N) : "memory"); }
DI void wait_lgkm0() { asm volatile("s_waitcnt lgkmcnt(0)" ::: "memory"); }
template <int N> DI void wait_lgkm() { asm volatile("s_waitcnt lgkmcnt(%0)" ::"n"(N > 15 ? 15 : N) : "memory"); }
DI void bar() { asm volatile("s_barrier" ::: "memory"); }
DI unsigned cvt_pk_bf16(float lo, float hi) {
  typedef __bf16 bf16x2 __attribute__((ext_vector_type(2)));
  f32x2 v = {lo, hi}; bf16x2 b = __builtin_convertvector(v, bf16x2); return __builtin_bit_cast(unsigned, b);
}
DI float bf_lo(unsigned w) { return __uint_as_float(w << 16); }
DI float bf_hi(unsigned w) { return __uint_as_float(w & 0xffff0000u); }
DI float rbf(float v) { return bf_lo(cvt_pk_bf16(v, v)); }
template <int I, int N, class F> DI void static_for(F&& f) { if constexpr (I < N) { f(std::integral_constant<int, I>{}); static_for<I + 1, N>(f); } }

#define ACLOB "a0","a1","a2","a3","a4","a5","a6","a7","a8","a9","a10","a11","a12","a13","a14","a15","a16","a17","a18","a19","a20","a21","a22","a23","a24","a25","a26","a27","a28","a29","a30","a31","a32","a33","a34","a35","a36","a37","a38","a39","a40","a41","a42","a43","a44","a45","a46","a47","a48","a49","a50","a51","a52","a53","a54","a55","a56","a57","a58","a59","a60","a61","a62","a63","a64","a65","a66","a67","a68","a69","a70","a71","a72","a73","a74","a75","a76","a77","a78","a79","a80","a81","a82","a83","a84","a85","a86","a87","a88","a89","a90","a91","a92","a93","a94","a95","a96","a97","a98","a99","a100","a101","a102","a103","a104","a105","a106","a107","a108","a109","a110","a111","a112","a113","a114","a115","a116","a117","a118","a119","a120","a121","a122","a123","a124","a125","a126","a127","a128","a129","a130","a131","a132","a133","a134","a135","a136","a137","a138","a139","a140","a141","a142","a143","a144","a145","a146","a147","a148","a149","a150","a151","a152","a153","a154","a155","a156","a157","a158","a159","a160","a161","a162","a163","a164","a165","a166","a167","a168","a169","a170","a171","a172","a173","a174","a175","a176","a177","a178","a179","a180","a181","a182","a183","a184","a185","a186","a187","a188","a189","a190","a191"
template <int RA, int RB> DI void mfma16(f32x4& acc) { asm volatile("v_mfma_f32_16x16x32_bf16 %0, a[%c1:%c2], a[%c3:%c4], %0" : "+v"(acc) : "n"(RA), "n"(RA + 3), "n"(RB), "n"(RB + 3)); }
template <int RD, int OFF> DI void ds_r128(unsigned addr) { asm volatile("ds_read_b128 a[%c1:%c2], %0 offset:%c3" ::"v"(addr), "n"(RD), "n"(RD + 3), "n"(OFF) : "memory"); }
template <int RD, int OFF> DI void ds_rtr(unsigned addr) { asm volatile("ds_read_b64_tr_b16 a[%c1:%c2], %0 offset:%c3" ::"v"(addr), "n"(RD), "n"(RD + 1), "n"(OFF) : "memory"); }
DI void wait_frags() { asm volatile("s_waitcnt lgkmcnt(0)" ::: "memory", ACLOB); }
template <int OFF, int POL> DI void dma16(int voff, __amdgpu_buffer_rsrc_t r, unsigned soff) {
  if constexpr (POL == 0) asm volatile("buffer_load_dwordx4 %0, %1, %2 offen offset:%3 lds" ::"v"(voff), "s"(r), "s"(soff), "n"(OFF) : "memory");
  if constexpr (POL == 1) asm volatile("buffer_load_dwordx4 %0, %1, %2 offen offset:%3 sc1 lds" ::"v"(voff), "s"(r), "s"(soff), "n"(OFF) : "memory");
  if constexpr (POL == 2) asm volatile("buffer_load_dwordx4 %0, %1, %2 offen offset:%3 nt lds" ::"v"(voff), "s"(r), "s"(soff), "n"(OFF) : "memory");
}
DI void pinacc(f32x4& v) { asm volatile("" : "+v"(v)); }
template <int C> DI int s_eq(int v) { int r; asm volatile("s_cmp_eq_u32 %1, %2\n\ts_cselect_b32 %0, 1, 0" : "=s"(r) : "s"(v), "n"(C) : "scc"); return r; }
DI __amdgpu_buffer_rsrc_t mkrsrc(const void* p, unsigned bytes) { return __builtin_amdgcn_make_buffer_rsrc((void*)p, 0, bytes, 0x00020000); }

#define STAMP(k, i) do {} while (0)
#define STAGGER 0
#define SNAKE 1
#define LN_PASS0 0
enum { ROWK = 0, FRAG = 1, NMAJ = 2 };

template <int KIND, int R, int POL> struct Stream {
  static constexpr bool ODD = KIND == NMAJ && R == 224;
  static constexpr int P = R / 32, NG = (P + 3) / 4, NV = ODD ? 7 : (KIND == FRAG ? 2 : 4);
  __amdgpu_buffer_rsrc_t rs;
  int voff[NV];
  unsigned s0[NG], ks;
  unsigned ml[NG];
  static DI int gpiece(int g, int wid) { if constexpr (ODD) return wid * 7 + 4 * g; else return 16 * g + 4 * wid; }
  DI void init_rowk(const void* p, unsigned bytes, int ld, int r0, int k0, int wid, int lane) {
    rs = mkrsrc(p, bytes);
#pragma unroll
    for (int j = 0; j < 4; ++j) voff[j] = ((lane >> 3) + 8 * j) * ld * 2 + (((lane & 7) ^ ((4 * (j & 1) + (lane >> 4)) & 7)) * 16) - j * 1024;
#pragma unroll
    for (int g = 0; g < NG; ++g) s0[g] = (unsigned)((r0 + 8 * gpiece(g, wid)) * ld + k0) * 2u;
    ks = 128u;
  }
  DI void init_frag(const void* p, unsigned bytes, int kbn, int r0, int k0, int wid, int lane) {
    rs = mkrsrc(p, bytes);
    voff[0] = lane * 16; voff[1] = lane * 16 + kbn * 1024 - 2048;
#pragma unroll
    for (int g = 0; g < NG; ++g) s0[g] = (unsigned)((r0 / 16 + gpiece(g, wid) / 2) * kbn + k0 / 32) * 1024u;
    ks = 2048u;
  }
  DI void init_nmaj(const void* p, unsigned bytes, int ld, int n0, int k0, int wid, int lane) {
    rs = mkrsrc(p, bytes);
    if constexpr (ODD) {
#pragma unroll
      for (int j = 0; j < 7; ++j) { const int L = (wid * 7 + j) * 64 + lane, kk = L / 28, pos = L % 28;
        voff[j] = kk * ld * 2 + (pos ^ (((kk >> 3) & 1) << 1)) * 16 - (j & 3) * 1024; }
      s0[0] = s0[1] = (unsigned)(k0 * ld + n0) * 2u;
    } else {
      static_assert(R == 128 || ODD, "");
#pragma unroll
      for (int j = 0; j < 4; ++j) { const int kk = 4 * (4 * wid + j) + (lane >> 4), g = (kk & 3) | (((kk >> 3) & 1) << 2);
        voff[j] = (4 * j + (lane >> 4)) * ld * 2 + (((lane & 15) ^ (g << 1)) * 16) - j * 1024; }
      s0[0] = (unsigned)((k0 + 16 * wid) * ld + n0) * 2u;
    }
    ks = (unsigned)(64 * ld) * 2u;
  }
  DI void init_lds(unsigned base, int wid) {
#pragma unroll
    for (int g = 0; g < NG; ++g) ml[g] = base + (unsigned)gpiece(g, wid) * 1024u;
  }
  template <int I, int SO> DI void m0() const { asm volatile("s_add_u32 m0, %0, %1" ::"s"(ml[I / 4]), "n"(SO) : "memory", "scc"); }
  template <int I> DI void issue() const {
    constexpr int g = I / 4, j = I % 4, vi = ODD ? I : (KIND == FRAG ? (j >> 1) : j);
    dma16<j * 1024, POL>(voff[vi], rs, s0[g]);
  }
  template <int I> DI void advance() { asm volatile("s_add_u32 %0, %0, %1" : "+s"(s0[I / 4]) : "s"(ks) : "scc"); }
};

template <int KIND, int R, int T, int NS, int STAGE> struct Reader {
  static constexpr int NA = KIND == FRAG ? 1 : (KIND == ROWK ? 2 : 3);
  static constexpr int SPAN = KIND == NMAJ ? 64 * R * 2 : T * 2048, SPW = (65536 - SPAN) / STAGE + 1, NW = (NS + SPW - 1) / SPW;
  unsigned a[NW][NA];
  DI void init(unsigned base, int t0, int lane) {
    const int fr = lane & 15, fq = lane >> 4;
    if constexpr (KIND == ROWK) { a[0][0] = base + (t0 * 16 + fr) * 128 + ((fq ^ (fr >> 1)) * 16); a[0][1] = base + (t0 * 16 + fr) * 128 + (((4 + fq) ^ (fr >> 1)) * 16); }
    else if constexpr (KIND == FRAG) a[0][0] = base + t0 * 2048 + lane * 16;
    else { static_assert(KIND != NMAJ || R == 224, "");
      const int q = fr >> 2, p = lane & 3, wn = t0 / 7; const unsigned b = base + (8 * fq + q) * 448 + 8 * p;
      a[0][0] = b + 32 * ((6 * wn) ^ (fq & 1)); a[0][1] = b + 32 * ((6 * wn + 1) ^ (fq & 1)); a[0][2] = b + 32 * ((12 + wn) ^ (fq & 1)); }
#pragma unroll
    for (int w = 1; w < NW; ++w)
#pragma unroll
      for (int n = 0; n < NA; ++n) { a[w][n] = a[0][n] + w * SPW * STAGE; asm volatile("" : "+v"(a[w][n])); }
  }
  template <int ST, int KS, int I, int RB> DI void rd() const {
    constexpr int w = ST / SPW, so = (ST % SPW) * STAGE;
    if constexpr (KIND == ROWK) ds_r128<RB + 4 * I, so + I * 2048>(a[w][KS]);
    else if constexpr (KIND == FRAG) ds_r128<RB + 4 * I, so + I * 2048 + KS * 1024>(a[w][0]);
    else { constexpr int n = I / 2, h = I % 2; ds_rtr<RB + 4 * n + 2 * h, so + (32 * KS + 4 * h) * (R * 2) + (n < 6 ? 32 * (n & ~1) : 0)>(a[w][n < 6 ? (n & 1) : 2]); }
  }
};

template <int BM_, int BN_, int NS_, int KA_, int KB_, int PLA_, int PLB_, bool TR_, int NX_, bool LAZY_, int RSP_> struct Geo {
  static constexpr bool LAZY = LAZY_; static constexpr int NX = NX_, PLA = PLA_, PLB = PLB_;
  static constexpr int BM = BM_, BN = BN_, NS = NS_, KA = KA_, KB = KB_, MT = BM / 32, NT = BN / 32, NM = MT * NT;
  static constexpr bool TR = TR_;
  static constexpr int PA = BM / 32, PB = BN / 32, ND = PA + PB, STAGE = (BM + BN) * 128;
  static constexpr int DEPTH = LAZY ? NS - 1 : NS;
  static constexpr int H = (DEPTH == 2) ? ND : ND / 2;
  static constexpr int NIA = KA == NMAJ ? 2 * MT : MT, NIB = KB == NMAJ ? 2 * NT : NT, NR = NIA + NIB, RSPAN = RSP_;
  static constexpr int rgap(int r) { return (r * RSPAN) / NR; }
  static constexpr int rbefore(int i) { int c = 0; for (int r = 0; r < NR; ++r) c += rgap(r) < i; return c; }
  typedef Reader<KA, BM, MT, NS, STAGE> RA; typedef Reader<KB, BN, NT, NS, STAGE> RB;
};
struct Side { __amdgpu_buffer_rsrc_t rs; unsigned voff, step, soff; };
template <class G> struct Pipe {
  Stream<G::KA, G::BM, G::PLA> A; Stream<G::KB, G::BN, G::PLB> B;
  Side xl, x2;
  unsigned ls = 0;
  unsigned* sig = nullptr; unsigned tag = 0;
  unsigned lds; int wid;
  DI void side() {
    if constexpr (G::NX > 0) asm volatile("buffer_load_dword a124, %0, %1, %2 offen" ::"v"(xl.voff), "s"(xl.rs), "s"(xl.soff) : "memory");
    if constexpr (G::NX > 1) asm volatile("buffer_load_dword a124, %0, %1, %2 offen" ::"v"(x2.voff), "s"(x2.rs), "s"(x2.soff) : "memory"); }
  DI void side_adv() {
    if constexpr (G::NX > 0) asm volatile("s_add_u32 %0, %0, %1" : "+s"(xl.soff) : "s"(xl.step) : "scc");
    if constexpr (G::NX > 1) asm volatile("s_add_u32 %0, %0, %1" : "+s"(x2.soff) : "s"(x2.step) : "scc"); }
  static constexpr bool gstart(int i) { return i < G::PA ? (i % 4 == 0) : ((i - G::PA) % 4 == 0); }
  static constexpr bool gend(int i) { return i == G::PA - 1 || i == G::ND - 1 || gstart(i + 1); }
  template <int I, int ST> DI void m0() const { if constexpr (I < G::PA) A.template m0<I, ST * G::STAGE>(); else B.template m0<I - G::PA, ST * G::STAGE>(); }
  template <int I> DI void issue() const { if constexpr (I < G::PA) A.template issue<I>(); else B.template issue<I - G::PA>(); }
  template <int I> DI void advance() { if constexpr (I < G::PA) A.template advance<I>(); else B.template advance<I - G::PA>(); }
  template <int MASK, bool LAST> DI void prologue() {
    if constexpr (MASK & 1) A.init_lds(lds, wid);
    if constexpr (MASK & 2) B.init_lds(lds + G::BM * 128, wid);
    asm volatile("s_nop 4" ::: "memory", ACLOB);
    static_for<0, G::DEPTH>([&](auto Ss) {
      if constexpr (LAST && Ss.value >= 2) { side(); side_adv(); }
      static_for<0, (Ss.value == G::DEPTH - 1 ? G::H : G::ND)>([&](auto Dd) { constexpr int d = Dd.value;
        if constexpr ((MASK >> (d < G::PA ? 0 : 1)) & 1) {
          if constexpr (gstart(d)) { m0<d, Ss.value>(); asm volatile("s_nop 0"); }
          issue<d>();
          if constexpr (gend(d)) advance<d>(); }
        else if constexpr (LAST && Ss.value >= 1) asm volatile("buffer_load_dword a124, off, %0, %1" ::"s"(A.rs), "s"(OOB) : "memory"); }); });
    if constexpr (LAST && G::H < G::ND) if constexpr (!gstart(G::H)) m0<G::H, G::DEPTH - 1>();
  }
};

constexpr int dgap(int d, int ndp, int nm, int w) { return 1 + (d * (nm - 2) + w * (nm - 2) / 4) / ndp; }
constexpr int dat(int i, int ndp, int nm, int w) { for (int d = 0; d < ndp; ++d) if (dgap(d, ndp, nm, w) == i) return d; return -1; }
template <class G, int W, int CUR, int RST, int RKS, int D0, int D1, int DST, int I>
DI void gap(Pipe<G>& p, const typename G::RA& ra, const typename G::RB& rb, f32x4 (&acc)[G::MT][G::NT]) {
  constexpr int CA = 64 * CUR, CB = CA + 32, NA = 64 * (CUR ^ 1), NB = NA + 32;
  constexpr int NM = G::NM, NIB = G::NIB, NR = G::NR, NDP = D1 - D0, m = I / G::NT, n0 = I % G::NT, n = (SNAKE && (m & 1)) ? G::NT - 1 - n0 : n0;
  constexpr int d = dat(I, NDP, NM, W), dp = dat(I - 1, NDP, NM, W);
  if constexpr (n0 == 0 && m % 2 == 0 && (G::LAZY || CUR == 0)) wait_lgkm<(NR - 1 - (NIB + m + 1)) + G::rbefore(I)>();
  if constexpr (d >= 0) if constexpr (Pipe<G>::gstart(D0 + d)) p.template m0<D0 + d, DST>();
  if constexpr (G::TR) mfma16<CB + 4 * n, CA + 4 * m>(acc[m][n]); else mfma16<CA + 4 * m, CB + 4 * n>(acc[m][n]);
  if constexpr (I == 0 && CUR == (G::DEPTH == 2 ? 0 : 1)) p.side();
  if constexpr (I == NM - 1 && CUR == (G::DEPTH == 2 ? 0 : 1)) p.side_adv();
  if constexpr (dp >= 0) if constexpr (Pipe<G>::gend(D0 + dp)) p.template advance<D0 + dp>();
  if constexpr (d >= 0) p.template issue<D0 + d>();
  static_for<G::rbefore(I), G::rbefore(I + 1)>([&](auto Rr) { constexpr int r = Rr.value;
    if constexpr (r == 0) ra.template rd<RST, RKS, 0, NA>();
    else if constexpr (r <= NIB) rb.template rd<RST, RKS, r - 1, NB>();
    else ra.template rd<RST, RKS, r - NIB, NA>(); });
}
template <class G, int W, int CUR, int RST, int RKS, int D0, int D1, int DST, int... Is>
DI void phase_(Pipe<G>& p, const typename G::RA& ra, const typename G::RB& rb, f32x4 (&acc)[G::MT][G::NT], std::integer_sequence<int, Is...>) {
  (gap<G, W, CUR, RST, RKS, D0, D1, DST, Is>(p, ra, rb, acc), ...);
}
template <class G, int W, int ST>
DI void ktile(Pipe<G>& p, const typename G::RA& ra, const typename G::RB& rb, f32x4 (&acc)[G::MT][G::NT]) {
  constexpr int NS = G::NS, ND = G::ND, H = G::H, DP = G::DEPTH;
  phase_<G, W, 0, ST, 1, H, ND, (ST + DP - 1) % NS>(p, ra, rb, acc, std::make_integer_sequence<int, G::NM>{});
  if constexpr (G::LAZY) asm volatile("s_waitcnt vmcnt(%0)\n\ts_barrier" ::"n"((DP - 2) * (ND + G::NX)) : "memory", ACLOB);
  else asm volatile("s_waitcnt vmcnt(%0) lgkmcnt(0)\n\ts_barrier" ::"n"(DP == 2 ? G::NX : (DP - 2) * (ND + G::NX)) : "memory", ACLOB);
  phase_<G, W, 1, (ST + 1) % NS, 0, 0, H, (ST + DP) % NS>(p, ra, rb, acc, std::make_integer_sequence<int, G::NM>{});
}
template <class G, int W, int NTILE, class Hook>
DI void gemm_body(Pipe<G>& p, const typename G::RA& ra, const typename G::RB& rb, f32x4 (&acc)[G::MT][G::NT], int on, Hook hook) {
#pragma nounroll
  for (int it = on * (NTILE / G::NS); it > 0; --it) { static_for<0, G::NS>([&](auto Ss) { ktile<G, W, Ss.value>(p, ra, rb, acc); });
    hook(it);
  }
  if constexpr (NTILE % G::NS != 0) {
    asm volatile("" : "+s"(on));
#pragma nounroll
    for (int it = on; it > 0; --it) static_for<0, NTILE % G::NS>([&](auto Ss) { ktile<G, W, Ss.value>(p, ra, rb, acc); });
  }
}
template <class G, int NTILE, class Init, class Hook>
DI void gemm_loop(Pipe<G>& p, int lane, f32x4 (&acc)[G::MT][G::NT], int st0, Init init, Hook hook) {
  constexpr int ND = G::ND, H = G::H, DP = G::DEPTH;
  typename G::RA ra; typename G::RB rb;
  ra.init(p.lds, (p.wid >> 1) * G::MT, lane); rb.init(p.lds + G::BM * 128, (p.wid & 1) * G::NT, lane);
#pragma unroll
  for (int n = 0; n < G::NT; ++n) { const f32x4 v = init(n);
#pragma unroll
    for (int m = 0; m < G::MT; ++m) { acc[m][n] = v; pinacc(acc[m][n]); } }
  wait_vm<(DP - 2) * (ND + G::NX) + H>();
  bar();
  if (p.sig != nullptr && threadIdx.x == 0) __hip_atomic_store(p.sig, p.tag, __ATOMIC_RELAXED, __HIP_MEMORY_SCOPE_AGENT);
  static_for<0, G::NIA>([&](auto I) { ra.template rd<0, 0, I.value, 0>(); });
  static_for<0, G::NIB>([&](auto I) { rb.template rd<0, 0, I.value, 32>(); });
  wait_frags();
  STAMP(1, st0);
  asm volatile("s_nop 1");
  gemm_body<G, 0, NTILE>(p, ra, rb, acc, 1, hook);
  asm volatile("s_nop 7\n\ts_nop 7\n\ts_waitcnt vmcnt(0) lgkmcnt(0)" ::: "memory", ACLOB);
#pragma unroll
  for (int m = 0; m < G::MT; ++m)
#pragma unroll
    for (int n = 0; n < G::NT; ++n) pinacc(acc[m][n]);
  STAMP(1, st0 + 1);
}

DI void touch(const void* p) { asm volatile("global_load_dword a124, %0, off" ::"v"(p) : "memory"); }
template <int R, int OFF> DI void gld4_nt(const void* p) { asm volatile("global_load_dwordx4 a[%c1:%c2], %0, off offset:%c3 nt" ::"v"(p), "n"(R), "n"(R + 3), "n"(OFF) : "memory"); }
template <int R> DI void gld2(const void* p) { asm volatile("global_load_dwordx2 a[%c1:%c2], %0, off" ::"v"(p), "n"(R), "n"(R + 1) : "memory"); }
template <int R, int OFF> DI void bld4_dev(__amdgpu_buffer_rsrc_t r, unsigned voff, unsigned soff) { asm volatile("buffer_load_dwordx4 a[%c3:%c4], %0, %1, %2 offen offset:%c5 sc1" ::"v"(voff), "s"(r), "s"(soff), "n"(R), "n"(R + 3), "n"(OFF) : "memory"); }
template <int R> DI void flag_req(const unsigned* p) { asm volatile("global_load_dword a[%c1], %0, off sc1" ::"v"(p), "n"(R) : "memory"); }
template <int R> DI unsigned ard() { unsigned r; asm volatile("v_accvgpr_read_b32 %0, a[%c1]" : "=v"(r) : "n"(R)); return r; }
template <int R> DI u32x4 ard4() { return (u32x4){ard<R>(), ard<R + 1>(), ard<R + 2>(), ard<R + 3>()}; }
template <int R> DI u32x2 ard2() { return (u32x2){ard<R>(), ard<R + 1>()}; }
template <int CTRL> DI float dpp_add(float a) { return a + __builtin_bit_cast(float, __builtin_amdgcn_update_dpp(0, __builtin_bit_cast(int, a), CTRL, 0xf, 0xf, true)); }
template <int N> DI void wave_sum(float (&s)[N]) {
#pragma unroll
  for (int r = 0; r < N; ++r) s[r] = dpp_add<0xB1>(s[r]);
#pragma unroll
  for (int r = 0; r < N; ++r) s[r] = dpp_add<0x4E>(s[r]);
#pragma unroll
  for (int r = 0; r < N; ++r) s[r] = dpp_add<0x141>(s[r]);
#pragma unroll
  for (int r = 0; r < N; ++r) s[r] = dpp_add<0x140>(s[r]);
#pragma unroll
  for (int r = 0; r < N; ++r) { const auto p = __builtin_amdgcn_permlane16_swap(__float_as_uint(s[r]), __float_as_uint(s[r]), false, false); s[r] = __uint_as_float(p[0]) + __uint_as_float(p[1]); }
#pragma unroll
  for (int r = 0; r < N; ++r) { const auto p = __builtin_amdgcn_permlane32_swap(__float_as_uint(s[r]), __float_as_uint(s[r]), false, false); s[r] = __uint_as_float(p[0]) + __uint_as_float(p[1]); }
}

DI f32x2 sigm_mul(f32x2 x, f32x2 arg) {
  f32x2 e; e.x = __builtin_amdgcn_exp2f(arg.x); e.y = __builtin_amdgcn_exp2f(arg.y);
  const f32x2 d = e + 1.0f;
  f32x2 r; r.x = __builtin_amdgcn_rcpf(d.x); r.y = __builtin_amdgcn_rcpf(d.y);
  return x * r;
}
DI f32x2 gelu2(f32x2 x) {
  const float k0 = -2.0f * 0.7978845608028654f * 1.4426950408889634f, k1 = k0 * 0.044715f;
  return sigm_mul(x, x * ((x * x) * k1 + k0));
}
DI f32x2 silu2(f32x2 x) { return sigm_mul(x, x * -1.4426950408889634f); }
DI f32x4 gelu4(f32x4 t) { const f32x2 a = gelu2((f32x2){t[0], t[1]}), b = gelu2((f32x2){t[2], t[3]}); return (f32x4){a.x, a.y, b.x, b.y}; }
DI f32x4 bf4(u32x2 w) { return (f32x4){bf_lo(w[0]), bf_hi(w[0]), bf_lo(w[1]), bf_hi(w[1])}; }

#define GO_NS 4
#define GO_LAZY false
#define GO_RSP 11
#define DEVPOL 1
typedef Geo<256, 128, 3, FRAG, ROWK, DEVPOL, 0, false, 1, false, 24> GAB;
#define YT1 3
#define YT2 11
#define ABT 4
#define PREP_DELAY 16
#define PREP_DELAY2 0
#define PREP_FIRST 0
#define EP1 1
#define EP2 5
#define ABS 8
#define YS0 5
#define YSN 5
#define WS2 1
#define HS0 3
#define HSN 8
typedef Geo<256, 128, 3, ROWK, FRAG, 0, DEVPOL, true, WS2 == 0, false, 24> GY;
#define H_TOUCH (ABT < 4)
typedef Geo<256, 224, 2, FRAG, NMAJ, DEVPOL, 0, true, WS2 == 1, false, 42> GH;
typedef Geo<128, 128, GO_NS, FRAG, FRAG, DEVPOL, DEVPOL, true, 0, GO_LAZY, GO_RSP> GO;
constexpr int LDSM = GY::NS * GY::STAGE > GO::NS * GO::STAGE ? GY::NS * GY::STAGE : GO::NS * GO::STAGE;

DI u32x4 pack_pair(const f32x4& a, const f32x4& b) {
  const unsigned a0 = cvt_pk_bf16(a[0], a[1]), a1 = cvt_pk_bf16(a[2], a[3]), b0 = cvt_pk_bf16(b[0], b[1]), b1 = cvt_pk_bf16(b[2], b[3]);
  const auto r0 = __builtin_amdgcn_permlane16_swap(a0, b0, false, false);
  const auto r1 = __builtin_amdgcn_permlane16_swap(a1, b1, false, false);
  return (u32x4){r0[0], r1[0], r0[1], r1[1]};
}
DI u32x4 pack8(const f32x4& a, const f32x4& b) { return (u32x4){cvt_pk_bf16(a[0], a[1]), cvt_pk_bf16(a[2], a[3]), cvt_pk_bf16(b[0], b[1]), cvt_pk_bf16(b[2], b[3])}; }
DI unsigned hpos(int t, int b) { return b == 0 ? 2 * t : (b == 3 ? 2 * t + 1 : 32 + 5 * t + (b < 3 ? b - 1 : b - 2)); }
struct NoHook { DI void operator()(int) const {} };
struct ZeroInit { DI f32x4 operator()(int) const { return (f32x4){0.f, 0.f, 0.f, 0.f}; } };

DI void st_dev(__amdgpu_buffer_rsrc_t r, unsigned off, u32x4 v) { __builtin_amdgcn_raw_buffer_store_b128(v, r, off, 0, 16); }
DI void st_dev8(__amdgpu_buffer_rsrc_t r, unsigned off, u32x2 v) { __builtin_amdgcn_raw_buffer_store_b64(v, r, off, 0, 16); }
DI u32x4 ld_dev(__amdgpu_buffer_rsrc_t r, unsigned voff, unsigned soff) { return __builtin_amdgcn_raw_buffer_load_b128(r, voff, soff, 16); }
DI void signal(unsigned* f, unsigned tag) { if (threadIdx.x == 0) __hip_atomic_store(f, tag, __ATOMIC_RELAXED, __HIP_MEMORY_SCOPE_AGENT); }
DI void publish(unsigned* f, unsigned tag) { wait_vm<0>(); bar(); signal(f, tag); }
DI void poll(unsigned* f, int n, unsigned tag) {
  unsigned* q = f + ((int)(threadIdx.x & 63) < n ? (threadIdx.x & 63) : 0) * FS;
#pragma nounroll
  for (int i = 0; i < SPIN_CAP && !__all(__hip_atomic_load(q, __ATOMIC_RELAXED, __HIP_MEMORY_SCOPE_AGENT) == tag); ++i) __builtin_amdgcn_s_sleep(1);
}
DI void await(unsigned* f, int n, unsigned tag) {
  if (threadIdx.x < 64) poll(f, n, tag);
  asm volatile("" ::: "memory");
  bar();
}

__global__ __launch_bounds__(256, 1) void k_main(const u16* __restrict__ x, const u16* __restrict__ ws_re, const u16* __restrict__ ws_im, const u16* __restrict__ wd_re, const u16* __restrict__ wd_im,
                                                 const u16* __restrict__ w1, const u16* __restrict__ b1, const u16* __restrict__ w2, const u16* __restrict__ b2, unsigned char* ws, u16* __restrict__ out) {
  extern __shared__ __attribute__((aligned(16))) unsigned char shm[];
  {
    const char* pc; unsigned len;
    asm volatile("s_getpc_b64 %0\n.Lkm_pc:\n\ts_mov_b32 %1, .Lkm_endy-.Lkm_pc\n\ts_max_i32 %1, %1, .Lkm_endo-.Lkm_pc" : "=s"(pc), "=s"(len) :: "scc");
    asm volatile("buffer_load_dword a124, %0, %1, 0 offen" ::"v"((((blockIdx.x >> 3) & 1) * 256 + threadIdx.x) * 128), "s"(mkrsrc(pc, len)) : "memory", ACLOB);
  }
  const int wid = __builtin_amdgcn_readfirstlane(threadIdx.x >> 6), lane = threadIdx.x & 63, fr = lane & 15, fq = lane >> 4, wm = wid >> 1, wn = wid & 1;
  const int xcd = blockIdx.x & 7, slot = blockIdx.x >> 3, chain = slot >> 4, s4 = slot & 15;
  const int pm1 = (xcd & 1) * 4 + (s4 & 3), pn1 = (xcd >> 1) * 4 + (s4 >> 2);
  const int half = xcd & 1, pm = (xcd >> 2) * 4 + (s4 & 3), pn = ((xcd >> 1) & 1) * 4 + (s4 >> 2), tile = pm * 8 + pn;
  const unsigned ldsb = (unsigned)(size_t)(LAS unsigned char*)shm;
  const int lo = ((fq & 1) * 2 + (fq >> 1)) * 256 + fr * 16;
  unsigned* const flag0 = (unsigned*)(ws + WS_FLAG);
#define FLAG(i) (flag0 + (i) * FS)
  const __amdgpu_buffer_rsrc_t ry = mkrsrc(ws + WS_PY + (size_t)tile * 131072, 131072);
  STAMP(1, 0);
  const int kb = xcd * 16 + s4;
  unsigned tag;
  {
    const u16* px = x + (size_t)(blockIdx.x * 8 + wid * 2) * D_ + lane * 8;
    asm volatile("global_load_dword a16, %0, off sc1" ::"v"(FLAG(FL_E)) : "memory");
    gld4_nt<0, 0>(px); gld4_nt<4, 1024>(px); gld4_nt<8, 2048>(px); gld4_nt<12, 3072>(px); }
  auto layernorm = [&](auto NY) {
    wait_vm<NY.value>();
    const u32x4 raw[4] = {ard4<0>(), ard4<4>(), ard4<8>(), ard4<12>()};
    const unsigned ev = ard<16>();
    f32x2 v[2][8]; float sm[2], mean[2];
#pragma unroll
    for (int i = 0; i < 16; ++i) v[i >> 3][i & 7] = (f32x2){bf_lo(raw[i >> 2][i & 3]), bf_hi(raw[i >> 2][i & 3])};
#pragma unroll
    for (int r = 0; r < 2; ++r) { const f32x2 a = ((v[r][0] + v[r][1]) + (v[r][2] + v[r][3])) + ((v[r][4] + v[r][5]) + (v[r][6] + v[r][7])); sm[r] = a.x + a.y; }
    wave_sum(sm);
#pragma unroll
    for (int r = 0; r < 2; ++r) { mean[r] = sm[r] * (1.0f / D_); f32x2 a = {0.f, 0.f};
#pragma unroll
      for (int e = 0; e < 8; ++e) { const f32x2 dd = v[r][e] - mean[r]; a += dd * dd; }
      sm[r] = a.x + a.y; }
    wave_sum(sm);
    LAS unsigned char* tl = (LAS unsigned char*)shm;
#pragma unroll
    for (int r = 0; r < 2; ++r) { const float rden = 1.0f / rbf(sqrtf(rbf(rbf(sm[r] * (1.0f / D_)) + 1e-5f))), mb = rbf(mean[r]);
#pragma unroll
      for (int j = 0; j < 2; ++j) { u32x4 o;
#pragma unroll
        for (int e = 0; e < 4; ++e) { const f32x2 d = v[r][4 * j + e] - mb; const unsigned db = cvt_pk_bf16(d.x, d.y); const f32x2 q = (f32x2){bf_lo(db), bf_hi(db)} * rden; o[e] = cvt_pk_bf16(q.x, q.y); }
        *(LAS u32x4*)(tl + (j * 64 + lane) * 128 + ((wid * 2 + r + lane) & 7) * 16) = o; } }
    tag = __builtin_amdgcn_readfirstlane(ev) + 1u;
    wait_lgkm0(); bar();
    STAMP(1, 14);
    const __amdgpu_buffer_rsrc_t rx = mkrsrc(ws + WS_XA + (size_t)(blockIdx.x >> 1) * 32768, 32768);
#pragma unroll
    for (int j = 0; j < 4; ++j) { const int ch = (wid * 4 + j) * 8 + (lane >> 3);
      st_dev(rx, ch * 256 + (blockIdx.x & 1) * 128 + (lane & 7) * 16, *(const LAS u32x4*)(tl + ch * 128 + ((lane + ch) & 7) * 16)); }
    publish(FLAG(FL_X + blockIdx.x), tag);
    STAMP(1, 10);
  };
  if (chain == 0) {
    const u16* wsm = half ? ws_im : ws_re;
    const u16* wshare = wsm + (size_t)(pm * 256 + pn * 32) * S_;
    f32x4 acc[8][4];
    {
      Pipe<GAB> p; p.lds = ldsb; p.wid = wid;
      p.B.init_rowk(pn1 < 8 ? wd_re : wd_im, D_ * D_ * 2, D_, (pn1 & 7) * 128, 0, wid, lane);
      p.prologue<2, false>();
      layernorm(std::integral_constant<int, 8>{});
      __builtin_amdgcn_s_sleep(PREP_DELAY);
      {
        const int t = (s4 * 8 + xcd) * 256 + threadIdx.x;
        const int c = (s4 & 3) * 256 + threadIdx.x;
        if (t < PREP_FIRST) touch((const char*)w1 + (size_t)(t + 7168) * 128);
        __builtin_amdgcn_s_sleep(PREP_DELAY2);
        touch((const char*)w1 + (size_t)t * 128);
        touch(t < 24576 ? (const char*)w1 + (size_t)(t + 32768) * 128 : (c < 512 ? (const char*)b1 + (c % 56) * 128 : (const char*)b2 + (c & 15) * 128));
        touch((const char*)(t < 16384 ? wd_re : wd_im) + (size_t)(t & 16383) * 128);
        const __amdgpu_buffer_rsrc_t rs = mkrsrc(w2 + (size_t)(kb < 112 ? kb : 0) * 32 * D_, kb < 112 ? 32 * D_ * 2 : 0);
        asm volatile("s_nop 4" ::: "memory");
        static_for<0, 16>([&](auto I) { constexpr int i = I.value;
          const int g = ((i >> 1) & 3) | ((wid & 1) << 2);
          asm volatile("s_add_u32 m0, %0, %1\n\ts_nop 0" ::"s"(ldsb + (1 + wm) * GAB::STAGE + wn * 16384), "n"(i * 1024) : "memory", "scc");
          dma16<0, 2>((lane ^ (g << 1)) * 16, rs, wid * 16384 + i * 1024); });
      }
      wait_vm<0>(); bar();
      if (kb < 112) {
        const int q = fr >> 2, gk = q | ((fq & 1) << 2);
        const __amdgpu_buffer_rsrc_t rw = mkrsrc(ws + WS_W2, F_ * D_ * 2);
        LAS unsigned char* src = (LAS unsigned char*)shm + (1 + (fq >> 1)) * GAB::STAGE + (8 * (fq & 1) + q) * 2048 + wid * 512 + 8 * (lane & 3);
        const unsigned kp = hpos(kb / 7, kb % 7);
#pragma unroll
        for (int n = 0; n < 16; ++n) {
          const bf16x4 l4 = __builtin_amdgcn_ds_read_tr16_b64_v4i16((LAS bf16x4*)(src + 32 * (n ^ gk)));
          const bf16x4 h4 = __builtin_amdgcn_ds_read_tr16_b64_v4i16((LAS bf16x4*)(src + 32 * (n ^ gk) + 8192));
          const u32x2 a = __builtin_bit_cast(u32x2, l4), b = __builtin_bit_cast(u32x2, h4);
          st_dev(rw, (unsigned)((wid * 16 + n) * 112 + kp) * 1024u + lane * 16, (u32x4){a[0], a[1], b[0], b[1]});
        }
        wait_lgkm0();
      }
      STAMP(1, 4);
      p.xl.rs = mkrsrc(wshare + (size_t)wid * 8 * S_, 32768); p.xl.step = 16384; p.xl.soff = 0u - (unsigned)(kb % ABS) * 16384u; p.xl.voff = fq * 4096 + fr * 128;
      p.A.init_frag(ws + WS_XA, S_ * D_ * 2, 32, pm1 * 256, 0, wid, lane);
      await(FLAG(FL_X + pm1 * 32), 32, tag);
      p.prologue<1, true>();
      p.sig = FLAG(FL_W2 + kb); p.tag = tag;
      gemm_loop<GAB, 16>(p, lane, acc, 1, ZeroInit(), NoHook());
      const __amdgpu_buffer_rsrc_t ro = mkrsrc(ws + WS_AB, S_ * 2 * D_ * 2);
      const unsigned ob = (unsigned)((pn1 * 8 + wn * 4) * 64 + pm1 * 8 + wm * 4) * 1024u + lo;
#pragma unroll
      for (int n = 0; n < 4; ++n)
#pragma unroll
        for (int m = 0; m < 8; m += 2) st_dev(ro, ob + (n * 64 + m / 2) * 1024, pack_pair(acc[m][n], acc[m + 1][n]));
      publish(FLAG(FL_AB + pn1 * 8 + pm1), tag);
      STAMP(1, 3);
    }
    {
      Pipe<GY> p; p.lds = ldsb; p.wid = wid;
      p.xl.rs = mkrsrc(wshare + (size_t)wid * 8 * S_, 32768); p.xl.step = 16384; p.xl.soff = 0u - (unsigned)(YS0 + kb % YSN) * 16384u; p.xl.voff = fq * 4096 + fr * 128 + 2048;
      p.A.init_rowk(wsm, S_ * S_ * 2, S_, pm * 256, 0, wid, lane);
      p.B.init_frag(ws + WS_AB, S_ * 2 * D_ * 2, 64, half * D_ + pn * 128, 0, wid, lane);
      p.prologue<1, false>();
      await(FLAG(FL_AB + (half * 8 + pn) * 8), 8, tag);
      STAMP(1, 5);
      p.prologue<2, true>();
      gemm_loop<GY, 32>(p, lane, acc, 6, ZeroInit(), NoHook());
#pragma unroll
      for (int i = 0; i < 16; ++i) st_dev(ry, lane * 16 + half * 65536 + wm * 32768 + (((i >> 3) * 2 + wn) * 8 + (i & 7)) * 1024, pack8(acc[i >> 1][(i & 1) * 2], acc[i >> 1][(i & 1) * 2 + 1]));
      asm volatile(".Lkm_endy:" ::: "memory");
      publish(FLAG(FL_Y + tile * 2 + half), tag);
      STAMP(1, 8);
    }
  } else {
    Pipe<GO> po; po.lds = ldsb; po.wid = wid;
    const size_t xo = (size_t)(pm * 256 + half * 128 + wm * 64 + (lane >> 3)) * D_ + pn * 128 + wn * 64 + (lane & 7) * 8;
    {
      const int c0 = pn1 * 224;
      Pipe<GH> p; p.lds = ldsb; p.wid = wid;
      p.xl.rs = mkrsrc((half ? ws_im : ws_re) + (size_t)(pm * 256 + pn * 32 + wid * 8) * S_, 32768); p.xl.step = 16384; p.xl.soff = 0u - (unsigned)(HS0 + kb % HSN) * 16384u; p.xl.voff = fq * 4096 + fr * 128 + 2048;
      static_for<0, 7>([&](auto N) { constexpr int n = N.value; gld2<32 + 2 * n>(b1 + c0 + (n < 6 ? wn * 96 + n * 16 : 192 + wn * 16) + fq * 4); });
      p.B.init_nmaj(w1, D_ * F_ * 2, F_, c0, 0, wid, lane);
      p.prologue<2, false>();
      layernorm(std::integral_constant<int, 21>{});
      p.A.init_frag(ws + WS_XA, S_ * D_ * 2, 32, pm1 * 256, 0, wid, lane);
      await(FLAG(FL_X + pm1 * 32), 32, tag);
      STAMP(1, 11);
      p.prologue<1, true>();
      f32x4 acc[8][7];
      const u32x2 bias[7] = {ard2<32>(), ard2<34>(), ard2<36>(), ard2<38>(), ard2<40>(), ard2<42>(), ard2<44>()};
      gemm_loop<GH, 16>(p, lane, acc, 1, [&](int n) { return bf4(bias[n]); }, NoHook());
      flag_req<125>(FLAG(FL_W2 + lane)); flag_req<126>(FLAG(FL_W2 + 64 + lane));
      const __amdgpu_buffer_rsrc_t ro = mkrsrc(ws + WS_H, S_ * F_ * 2);
      const unsigned ob = (unsigned)((pm1 * 16 + wm * 8) * 112) * 1024u, o1 = ob + (2 * pn1 + wn) * 1024 + lo, o2 = ob + (32 + 5 * pn1 + 2 * wn) * 1024 + lo,
                     os = ob + (32 + 5 * pn1 + 4) * 1024 + (wn * 2 + (fq >> 1)) * 256 + fr * 16 + (fq & 1) * 8;
#pragma unroll
      for (int m = 0; m < 8; ++m) st_dev(ro, o1 + m * 112 * 1024, pack_pair(gelu4(acc[m][0]), gelu4(acc[m][1])));
      asm volatile("" ::: "memory");
#pragma unroll
      for (int m = 0; m < 8; ++m) {
        st_dev(ro, o2 + m * 112 * 1024, pack_pair(gelu4(acc[m][2]), gelu4(acc[m][3])));
        st_dev(ro, o2 + m * 112 * 1024 + 1024, pack_pair(gelu4(acc[m][4]), gelu4(acc[m][5])));
        const f32x4 g = gelu4(acc[m][6]); st_dev8(ro, os + m * 112 * 1024, (u32x2){cvt_pk_bf16(g[0], g[1]), cvt_pk_bf16(g[2], g[3])});
        if (m == EP1) {
          wait_vm<3 * (EP1 + 1)>(); bar(); signal(FLAG(FL_H + pm1 * 32 + pn1), tag);
          if (!__all(ard<125>() == tag && ard<126>() == tag)) { poll(FLAG(FL_W2), 64, tag); poll(FLAG(FL_W2 + 64), 64, tag); }
          po.B.init_frag(ws + WS_W2, F_ * D_ * 2, 112, pn * 128, 0, wid, lane);
          po.prologue<2, false>();
          po.A.init_frag(ws + WS_H, S_ * F_ * 2, 112, pm * 256 + half * 128, 0, wid, lane);
        }
        if (m == EP2 - 1) flag_req<125>(FLAG(FL_H + pm * 32 + (lane & 15)));
        if (m == EP2) {
          wait_vm<3>();
          if (!__all(ard<125>() == tag)) poll(FLAG(FL_H + pm * 32), 16, tag);
          asm volatile("" ::: "memory");
          STAMP(1, 5);
          po.prologue<1, true>();
        }
      }
      STAMP(1, 3);
    }
    f32x4 acc[4][4];
    unsigned* fh2 = FLAG(FL_H + pm * 32 + 16);
    unsigned* fy = FLAG(FL_Y + tile * 2);
    auto yreq = [&]() {
      static_for<0, 16>([&](auto I) { constexpr int i = I.value;
        bld4_dev<128 + 4 * i, (i & 3) * 1024>(ry, lane * 16, (i >> 3) * 65536 + half * 32768 + wid * 8192 + (i & 4) * 1024); });
    };
    gemm_loop<GO, 56>(po, lane, acc, 6, ZeroInit(), [&](int it) {
      if (it == 14) signal(FLAG(FL_H + pm1 * 32 + 16 + pn1), tag);
      if (it == 13) flag_req<125>(fh2 + (lane & 15) * FS);
      if (it == 12) { if (!__all(ard<125>() == tag)) poll(fh2, 16, tag); asm volatile("" ::: "memory"); }
      if (it == 2) { flag_req<125>(fy + (lane & 1) * FS); static_for<0, 4>([&](auto N) { gld2<80 + 2 * N.value>(b2 + pn * 128 + wn * 64 + N.value * 16 + fq * 4); }); }
    });
    if (!__all(ard<125>() == tag)) poll(fy, 2, tag);
    asm volatile("" ::: "memory");
    yreq();
    static_for<0, 8>([&](auto J) { constexpr int j = J.value; gld4_nt<16 + 4 * (j & 3) + 32 * (j >> 2), 0>(x + xo + (size_t)j * 8 * D_); });
    STAMP(1, 8);
    const u32x2 bias[4] = {ard2<80>(), ard2<82>(), ard2<84>(), ard2<86>()};
#pragma unroll
    for (int i = 0; i < 16; ++i) { const f32x4 t = acc[i >> 2][i & 3] + bf4(bias[i & 3]);
      const f32x2 a = silu2((f32x2){t[0], t[1]}), b = silu2((f32x2){t[2], t[3]}); acc[i >> 2][i & 3] = (f32x4){a.x, a.y, b.x, b.y}; pinacc(acc[i >> 2][i & 3]); }
    wait_vm<0>(); bar();
    const u32x4 xr[8] = {ard4<16>(), ard4<20>(), ard4<24>(), ard4<28>(), ard4<48>(), ard4<52>(), ard4<56>(), ard4<60>()};
    u32x4 yr[8], yi[8];
    static_for<0, 8>([&](auto I) { yr[I.value] = ard4<128 + 4 * I.value>(); yi[I.value] = ard4<160 + 4 * I.value>(); });
    LAS unsigned char* tl = (LAS unsigned char*)shm + wid * (64 * 144);
#pragma unroll
    for (int i = 0; i < 8; ++i) {
      unsigned r[4];
#pragma unroll
      for (int e = 0; e < 4; ++e) { const f32x4 g = acc[i >> 1][(i & 1) * 2 + (e >> 1)];
        r[e] = cvt_pk_bf16((bf_lo(yr[i][e]) - bf_lo(yi[i][e])) * g[(e & 1) * 2], (bf_hi(yr[i][e]) - bf_hi(yi[i][e])) * g[(e & 1) * 2 + 1]); }
      LAS unsigned char* q = tl + ((i >> 1) * 16 + fr) * 144 + (i & 1) * 64 + fq * 8;
      *(LAS u32x2*)q = (u32x2){r[0], r[1]}; *(LAS u32x2*)(q + 32) = (u32x2){r[2], r[3]};
    }
    wait_lgkm0();
#pragma unroll
    for (int j = 0; j < 8; ++j) {
      const u32x4 r = *(const LAS u32x4*)(tl + (j * 8 + (lane >> 3)) * 144 + (lane & 7) * 16);
      u32x4 o;
#pragma unroll
      for (int e = 0; e < 4; ++e) o[e] = cvt_pk_bf16(bf_lo(xr[j][e]) + bf_lo(r[e]), bf_hi(xr[j][e]) + bf_hi(r[e]));
      __builtin_nontemporal_store(o, (u32x4*)(out + xo + (size_t)j * 8 * D_));
    }
    asm volatile(".Lkm_endo:" ::: "memory");
    if (blockIdx.x == 255) {
      poll(FLAG(FL_X + wid * 64), 64, tag);
      asm volatile("" ::: "memory"); bar();
      signal(FLAG(FL_E), tag);
    }
    STAMP(1, 9);
  }
}

extern "C" void kernel_launch(void* const* d_in, const int* in_sizes, int n_in, void* d_out, int out_size, void* d_ws, size_t ws_size, hipStream_t stream) {
  const u16* x = (const u16*)d_in[0]; const u16* ws_re = (const u16*)d_in[1]; const u16* ws_im = (const u16*)d_in[2];
  const u16* wd_re = (const u16*)d_in[3]; const u16* wd_im = (const u16*)d_in[4];
  const u16* w1 = (const u16*)d_in[6]; const u16* b1 = (const u16*)d_in[7]; const u16* w2 = (const u16*)d_in[8]; const u16* b2 = (const u16*)d_in[9];
  unsigned char* ws = (unsigned char*)d_ws;
  (void)hipFuncSetAttribute((const void*)k_main, hipFuncAttributeMaxDynamicSharedMemorySize, LDSM);
  k_main<<<256, 256, LDSM, stream>>>(x, ws_re, ws_im, wd_re, wd_im, w1, b1, w2, b2, ws, (u16*)d_out);
}
```

```cpp
#include <hip/hip_runtime.h>
#include <hip/hip_bf16.h>
#include <type_traits>
#include <utility>

#define LAS __attribute__((address_space(3)))
#define DI __device__ __forceinline__
typedef unsigned short u16;
typedef short bf16x4 __attribute__((ext_vector_type(4)));
typedef float f32x4 __attribute__((ext_vector_type(4)));
typedef float f32x2 __attribute__((ext_vector_type(2)));
typedef unsigned u32x4 __attribute__((ext_vector_type(4)));
typedef unsigned u32x2 __attribute__((ext_vector_type(2)));

constexpr int S_ = 2048, D_ = 1024, F_ = 3584;
constexpr size_t WS_FLAG = 0;
#define FS_ 16
constexpr int FS = FS_, FL_E = 0, FL_X = 32, FL_AB = FL_X + 256, FL_H = FL_AB + 128, FL_W2 = FL_H + 256, FL_Y = FL_W2 + 128;
constexpr size_t WS_XA = 262144;
constexpr size_t WS_AB = WS_XA + (size_t)S_ * D_ * 2;
constexpr size_t WS_H = WS_AB + (size_t)S_ * 2 * D_ * 2;
constexpr size_t WS_W2 = WS_H + (size_t)S_ * F_ * 2;
constexpr size_t WS_PY = WS_W2 + (size_t)F_ * D_ * 2;
constexpr unsigned OOB = 0x40000000u;
constexpr int SPIN_CAP = 1 << 20;

template <int N> DI void wait_vm() { asm volatile("s_waitcnt vmcnt(%0)" ::"n"(N) : "memory"); }
DI void wait_lgkm0() { asm volatile("s_waitcnt lgkmcnt(0)" ::: "memory"); }
template <int N> DI void wait_lgkm() { asm volatile("s_waitcnt lgkmcnt(%0)" ::"n"(N > 15 ? 15 : N) : "memory"); }
DI void bar() { asm volatile("s_barrier" ::: "memory"); }
DI unsigned cvt_pk_bf16(float lo, float hi) {
  typedef __bf16 bf16x2 __attribute__((ext_vector_type(2)));
  f32x2 v = {lo, hi}; bf16x2 b = __builtin_convertvector(v, bf16x2); return __builtin_bit_cast(unsigned, b);
}
DI float bf_lo(unsigned w) { return __uint_as_float(w << 16); }
DI float bf_hi(unsigned w) { return __uint_as_float(w & 0xffff0000u); }
DI float rbf(float v) { return bf_lo(cvt_pk_bf16(v, v)); }
template <int I, int N, class F> DI void static_for(F&& f) { if constexpr (I < N) { f(std::integral_constant<int, I>{}); static_for<I + 1, N>(f); } }

#define ACLOB "a0","a1","a2","a3","a4","a5","a6","a7","a8","a9","a10","a11","a12","a13","a14","a15","a16","a17","a18","a19","a20","a21","a22","a23","a24","a25","a26","a27","a28","a29","a30","a31","a32","a33","a34","a35","a36","a37","a38","a39","a40","a41","a42","a43","a44","a45","a46","a47","a48","a49","a50","a51","a52","a53","a54","a55","a56","a57","a58","a59","a60","a61","a62","a63","a64","a65","a66","a67","a68","a69","a70","a71","a72","a73","a74","a75","a76","a77","a78","a79","a80","a81","a82","a83","a84","a85","a86","a87","a88","a89","a90","a91","a92","a93","a94","a95","a96","a97","a98","a99","a100","a101","a102","a103","a104","a105","a106","a107","a108","a109","a110","a111","a112","a113","a114","a115","a116","a117","a118","a119","a120","a121","a122","a123","a124","a125","a126","a127","a128","a129","a130","a131","a132","a133","a134","a135","a136","a137","a138","a139","a140","a141","a142","a143","a144","a145","a146","a147","a148","a149","a150","a151","a152","a153","a154","a155","a156","a157","a158","a159","a160","a161","a162","a163","a164","a165","a166","a167","a168","a169","a170","a171","a172","a173","a174","a175","a176","a177","a178","a179","a180","a181","a182","a183","a184","a185","a186","a187","a188","a189","a190","a191"
template <int RA, int RB> DI void mfma16(f32x4& acc) { asm volatile("v_mfma_f32_16x16x32_bf16 %0, a[%c1:%c2], a[%c3:%c4], %0" : "+v"(acc) : "n"(RA), "n"(RA + 3), "n"(RB), "n"(RB + 3)); }
template <int RD, int OFF> DI void ds_r128(unsigned addr) { asm volatile("ds_read_b128 a[%c1:%c2], %0 offset:%c3" ::"v"(addr), "n"(RD), "n"(RD + 3), "n"(OFF) : "memory"); }
template <int RD, int OFF> DI void ds_rtr(unsigned addr) { asm volatile("ds_read_b64_tr_b16 a[%c1:%c2], %0 offset:%c3" ::"v"(addr), "n"(RD), "n"(RD + 1), "n"(OFF) : "memory"); }
DI void wait_frags() { asm volatile("s_waitcnt lgkmcnt(0)" ::: "memory", ACLOB); }
template <int OFF, int POL> DI void dma16(int voff, __amdgpu_buffer_rsrc_t r, unsigned soff) {
  if constexpr (POL == 0) asm volatile("buffer_load_dwordx4 %0, %1, %2 offen offset:%3 lds" ::"v"(voff), "s"(r), "s"(soff), "n"(OFF) : "memory");
  if constexpr (POL == 1) asm volatile("buffer_load_dwordx4 %0, %1, %2 offen offset:%3 sc1 lds" ::"v"(voff), "s"(r), "s"(soff), "n"(OFF) : "memory");
  if constexpr (POL == 2) asm volatile("buffer_load_dwordx4 %0, %1, %2 offen offset:%3 nt lds" ::"v"(voff), "s"(r), "s"(soff), "n"(OFF) : "memory");
}
DI void pinacc(f32x4& v) { asm volatile("" : "+v"(v)); }
template <int C> DI int s_eq(int v) { int r; asm volatile("s_cmp_eq_u32 %1, %2\n\ts_cselect_b32 %0, 1, 0" : "=s"(r) : "s"(v), "n"(C) : "scc"); return r; }
DI __amdgpu_buffer_rsrc_t mkrsrc(const void* p, unsigned bytes) { return __builtin_amdgcn_make_buffer_rsrc((void*)p, 0, bytes, 0x00020000); }

#define STAMP(k, i) do {} while (0)
#define STAGGER 0
#define SNAKE 1
#define LN_PASS0 0
enum { ROWK = 0, FRAG = 1, NMAJ = 2 };

template <int KIND, int R, int POL> struct Stream {
  static constexpr bool ODD = KIND == NMAJ && R == 224;
  static constexpr int P = R / 32, NG = (P + 3) / 4, NV = ODD ? 7 : (KIND == FRAG ? 2 : 4);
  __amdgpu_buffer_rsrc_t rs;
  int voff[NV];
  unsigned s0[NG], ks;
  unsigned ml[NG];
  static DI int gpiece(int g, int wid) { if constexpr (ODD) return wid * 7 + 4 * g; else return 16 * g + 4 * wid; }
  DI void init_rowk(const void* p, unsigned bytes, int ld, int r0, int k0, int wid, int lane) {
    rs = mkrsrc(p, bytes);
#pragma unroll
    for (int j = 0; j < 4; ++j) voff[j] = ((lane >> 3) + 8 * j) * ld * 2 + (((lane & 7) ^ ((4 * (j & 1) + (lane >> 4)) & 7)) * 16) - j * 1024;
#pragma unroll
    for (int g = 0; g < NG; ++g) s0[g] = (unsigned)((r0 + 8 * gpiece(g, wid)) * ld + k0) * 2u;
    ks = 128u;
  }
  DI void init_frag(const void* p, unsigned bytes, int kbn, int r0, int k0, int wid, int lane) {
    rs = mkrsrc(p, bytes);
    voff[0] = lane * 16; voff[1] = lane * 16 + kbn * 1024 - 2048;
#pragma unroll
    for (int g = 0; g < NG; ++g) s0[g] = (unsigned)((r0 / 16 + gpiece(g, wid) / 2) * kbn + k0 / 32) * 1024u;
    ks = 2048u;
  }
  DI void init_nmaj(const void* p, unsigned bytes, int ld, int n0, int k0, int wid, int lane) {
    rs = mkrsrc(p, bytes);
    if constexpr (ODD) {
#pragma unroll
      for (int j = 0; j < 7; ++j) { const int L = (wid * 7 + j) * 64 + lane, kk = L / 28, pos = L % 28;
        voff[j] = kk * ld * 2 + (pos ^ (((kk >> 3) & 1) << 1)) * 16 - (j & 3) * 1024; }
      s0[0] = s0[1] = (unsigned)(k0 * ld + n0) * 2u;
    } else {
      static_assert(R == 128 || ODD, "");
#pragma unroll
      for (int j = 0; j < 4; ++j) { const int kk = 4 * (4 * wid + j) + (lane >> 4), g = (kk & 3) | (((kk >> 3) & 1) << 2);
        voff[j] = (4 * j + (lane >> 4)) * ld * 2 + (((lane & 15) ^ (g << 1)) * 16) - j * 1024; }
      s0[0] = (unsigned)((k0 + 16 * wid) * ld + n0) * 2u;
    }
    ks = (unsigned)(64 * ld) * 2u;
  }
  DI void init_lds(unsigned base, int wid) {
#pragma unroll
    for (int g = 0; g < NG; ++g) ml[g] = base + (unsigned)gpiece(g, wid) * 1024u;
  }
  template <int I, int SO> DI void m0() const { asm volatile("s_add_u32 m0, %0, %1" ::"s"(ml[I / 4]), "n"(SO) : "memory", "scc"); }
  template <int I> DI void issue() const {
    constexpr int g = I / 4, j = I % 4, vi = ODD ? I : (KIND == FRAG ? (j >> 1) : j);
    dma16<j * 1024, POL>(voff[vi], rs, s0[g]);
  }
  unsigned se[NG];
  DI void init_end(int nt) {
#pragma unroll
    for (int g = 0; g < NG; ++g) se[g] = s0[g] + nt * ks;
  }
  template <int I> DI void advance() { asm volatile("s_add_u32 %0, %0, %1\n\ts_cmp_eq_u32 %0, %2\n\ts_cselect_b32 %0, 2.0, %0" : "+s"(s0[I / 4]) : "s"(ks), "s"(se[I / 4]) : "scc"); }
};

template <int KIND, int R, int T, int NS, int STAGE> struct Reader {
  static constexpr int NA = KIND == FRAG ? 1 : (KIND == ROWK ? 2 : 3);
  static constexpr int SPAN = KIND == NMAJ ? 64 * R * 2 : T * 2048, SPW = (65536 - SPAN) / STAGE + 1, NW = (NS + SPW - 1) / SPW;
  unsigned a[NW][NA];
  DI void init(unsigned base, int t0, int lane) {
    const int fr = lane & 15, fq = lane >> 4;
    if constexpr (KIND == ROWK) { a[0][0] = base + (t0 * 16 + fr) * 128 + ((fq ^ (fr >> 1)) * 16); a[0][1] = base + (t0 * 16 + fr) * 128 + (((4 + fq) ^ (fr >> 1)) * 16); }
    else if constexpr (KIND == FRAG) a[0][0] = base + t0 * 2048 + lane * 16;
    else { static_assert(KIND != NMAJ || R == 224, "");
      const int q = fr >> 2, p = lane & 3, wn = t0 / 7; const unsigned b = base + (8 * fq + q) * 448 + 8 * p;
      a[0][0] = b + 32 * ((6 * wn) ^ (fq & 1)); a[0][1] = b + 32 * ((6 * wn + 1) ^ (fq & 1)); a[0][2] = b + 32 * ((12 + wn) ^ (fq & 1)); }
#pragma unroll
    for (int w = 1; w < NW; ++w)
#pragma unroll
      for (int n = 0; n < NA; ++n) { a[w][n] = a[0][n] + w * SPW * STAGE; asm volatile("" : "+v"(a[w][n])); }
  }
  template <int ST, int KS, int I, int RB> DI void rd() const {
    constexpr int w = ST / SPW, so = (ST % SPW) * STAGE;
    if constexpr (KIND == ROWK) ds_r128<RB + 4 * I, so + I * 2048>(a[w][KS]);
    else if constexpr (KIND == FRAG) ds_r128<RB + 4 * I, so + I * 2048 + KS * 1024>(a[w][0]);
    else { constexpr int n = I / 2, h = I % 2; ds_rtr<RB + 4 * n + 2 * h, so + (32 * KS + 4 * h) * (R * 2) + (n < 6 ? 32 * (n & ~1) : 0)>(a[w][n < 6 ? (n & 1) : 2]); }
  }
};

template <int NTILE_, int BM_, int BN_, int NS_, int KA_, int KB_, int PLA_, int PLB_, bool TR_, int NX_, bool LAZY_, int RSP_> struct Geo {
  static constexpr bool LAZY = LAZY_; static constexpr int NTILE = NTILE_, NX = NX_, PLA = PLA_, PLB = PLB_;
  static constexpr int BM = BM_, BN = BN_, NS = NS_, KA = KA_, KB = KB_, MT = BM / 32, NT = BN / 32, NM = MT * NT;
  static constexpr bool TR = TR_;
  static constexpr int PA = BM / 32, PB = BN / 32, ND = PA + PB, STAGE = (BM + BN) * 128;
  static constexpr int DEPTH = LAZY ? NS - 1 : NS;
  static constexpr int H = (DEPTH == 2) ? ND : ND / 2;
  static constexpr int NIA = KA == NMAJ ? 2 * MT : MT, NIB = KB == NMAJ ? 2 * NT : NT, NR = NIA + NIB, RSPAN = RSP_;
  static constexpr int rgap(int r) { return (r * RSPAN) / NR; }
  static constexpr int rbefore(int i) { int c = 0; for (int r = 0; r < NR; ++r) c += rgap(r) < i; return c; }
  typedef Reader<KA, BM, MT, NS, STAGE> RA; typedef Reader<KB, BN, NT, NS, STAGE> RB;
};
struct Side { __amdgpu_buffer_rsrc_t rs; unsigned voff, step, soff; };
template <class G> struct Pipe {
  Stream<G::KA, G::BM, G::PLA> A; Stream<G::KB, G::BN, G::PLB> B;
  Side xl, x2;
  unsigned ls = 0;
  unsigned* sig = nullptr; unsigned tag = 0;
  unsigned lds; int wid;
  DI void side() {
    if constexpr (G::NX > 0) asm volatile("buffer_load_dword a124, %0, %1, %2 offen" ::"v"(xl.voff), "s"(xl.rs), "s"(xl.soff) : "memory");
    if constexpr (G::NX > 1) asm volatile("buffer_load_dword a124, %0, %1, %2 offen" ::"v"(x2.voff), "s"(x2.rs), "s"(x2.soff) : "memory"); }
  DI void side_adv() {
    if constexpr (G::NX > 0) asm volatile("s_add_u32 %0, %0, %1" : "+s"(xl.soff) : "s"(xl.step) : "scc");
    if constexpr (G::NX > 1) asm volatile("s_add_u32 %0, %0, %1" : "+s"(x2.soff) : "s"(x2.step) : "scc"); }
  static constexpr bool gstart(int i) { return i < G::PA ? (i % 4 == 0) : ((i - G::PA) % 4 == 0); }
  static constexpr bool gend(int i) { return i == G::PA - 1 || i == G::ND - 1 || gstart(i + 1); }
  template <int I, int ST> DI void m0() const { if constexpr (I < G::PA) A.template m0<I, ST * G::STAGE>(); else B.template m0<I - G::PA, ST * G::STAGE>(); }
  template <int I> DI void issue() const { if constexpr (I < G::PA) A.template issue<I>(); else B.template issue<I - G::PA>(); }
  template <int I> DI void advance() { if constexpr (I < G::PA) A.template advance<I>(); else B.template advance<I - G::PA>(); }
  template <int MASK, bool LAST> DI void prologue() {
    if constexpr (MASK & 1) { A.init_lds(lds, wid); A.init_end(G::NTILE); }
    if constexpr (MASK & 2) { B.init_lds(lds + G::BM * 128, wid); B.init_end(G::NTILE); }
    asm volatile("s_nop 4" ::: "memory", ACLOB);
    static_for<0, G::DEPTH>([&](auto Ss) {
      if constexpr (LAST && Ss.value >= 2) { side(); side_adv(); }
      static_for<0, (Ss.value == G::DEPTH - 1 ? G::H : G::ND)>([&](auto Dd) { constexpr int d = Dd.value;
        if constexpr ((MASK >> (d < G::PA ? 0 : 1)) & 1) {
          if constexpr (gstart(d)) { m0<d, Ss.value>(); asm volatile("s_nop 0"); }
          issue<d>();
          if constexpr (gend(d)) advance<d>(); }
        else if constexpr (LAST && Ss.value >= 1) asm volatile("buffer_load_dword a124, off, %0, %1" ::"s"(A.rs), "s"(OOB) : "memory"); }); });
    if constexpr (LAST && G::H < G::ND) if constexpr (!gstart(G::H)) m0<G::H, G::DEPTH - 1>();
  }
};

constexpr int dgap(int d, int ndp, int nm, int w) { return 1 + (d * (nm - 2) + w * (nm - 2) / 4) / ndp; }
constexpr int dat(int i, int ndp, int nm, int w) { for (int d = 0; d < ndp; ++d) if (dgap(d, ndp, nm, w) == i) return d; return -1; }
template <class G, int W, int CUR, int RST, int RKS, int D0, int D1, int DST, int I>
DI void gap(Pipe<G>& p, const typename G::RA& ra, const typename G::RB& rb, f32x4 (&acc)[G::MT][G::NT]) {
  constexpr int CA = 64 * CUR, CB = CA + 32, NA = 64 * (CUR ^ 1), NB = NA + 32;
  constexpr int NM = G::NM, NIB = G::NIB, NR = G::NR, NDP = D1 - D0, m = I / G::NT, n0 = I % G::NT, n = (SNAKE && (m & 1)) ? G::NT - 1 - n0 : n0;
  constexpr int d = dat(I, NDP, NM, W), dp = dat(I - 1, NDP, NM, W);
  if constexpr (n0 == 0 && m % 2 == 0 && (G::LAZY || CUR == 0)) wait_lgkm<(NR - 1 - (NIB + m + 1)) + G::rbefore(I)>();
  if constexpr (d >= 0) if constexpr (Pipe<G>::gstart(D0 + d)) p.template m0<D0 + d, DST>();
  if constexpr (G::TR) mfma16<CB + 4 * n, CA + 4 * m>(acc[m][n]); else mfma16<CA + 4 * m, CB + 4 * n>(acc[m][n]);
  if constexpr (I == 0 && CUR == (G::DEPTH == 2 ? 0 : 1)) p.side();
  if constexpr (I == NM - 1 && CUR == (G::DEPTH == 2 ? 0 : 1)) p.side_adv();
  if constexpr (dp >= 0) if constexpr (Pipe<G>::gend(D0 + dp)) p.template advance<D0 + dp>();
  if constexpr (d >= 0) p.template issue<D0 + d>();
  static_for<G::rbefore(I), G::rbefore(I + 1)>([&](auto Rr) { constexpr int r = Rr.value;
    if constexpr (r == 0) ra.template rd<RST, RKS, 0, NA>();
    else if constexpr (r <= NIB) rb.template rd<RST, RKS, r - 1, NB>();
    else ra.template rd<RST, RKS, r - NIB, NA>(); });
}
template <class G, int W, int CUR, int RST, int RKS, int D0, int D1, int DST, int... Is>
DI void phase_(Pipe<G>& p, const typename G::RA& ra, const typename G::RB& rb, f32x4 (&acc)[G::MT][G::NT], std::integer_sequence<int, Is...>) {
  (gap<G, W, CUR, RST, RKS, D0, D1, DST, Is>(p, ra, rb, acc), ...);
}
template <class G, int W, int ST>
DI void ktile(Pipe<G>& p, const typename G::RA& ra, const typename G::RB& rb, f32x4 (&acc)[G::MT][G::NT]) {
  constexpr int NS = G::NS, ND = G::ND, H = G::H, DP = G::DEPTH;
  phase_<G, W, 0, ST, 1, H, ND, (ST + DP - 1) % NS>(p, ra, rb, acc, std::make_integer_sequence<int, G::NM>{});
  if constexpr (G::LAZY) asm volatile("s_waitcnt vmcnt(%0)\n\ts_barrier" ::"n"((DP - 2) * (ND + G::NX)) : "memory", ACLOB);
  else asm volatile("s_waitcnt vmcnt(%0) lgkmcnt(0)\n\ts_barrier" ::"n"(DP == 2 ? G::NX : (DP - 2) * (ND + G::NX)) : "memory", ACLOB);
  phase_<G, W, 1, (ST + 1) % NS, 0, 0, H, (ST + DP) % NS>(p, ra, rb, acc, std::make_integer_sequence<int, G::NM>{});
}
template <class G, int W, int NTILE, class Hook>
DI void gemm_body(Pipe<G>& p, const typename G::RA& ra, const typename G::RB& rb, f32x4 (&acc)[G::MT][G::NT], int on, Hook hook) {
#pragma nounroll
  for (int it = on * (NTILE / G::NS); it > 0; --it) { static_for<0, G::NS>([&](auto Ss) { ktile<G, W, Ss.value>(p, ra, rb, acc); });
    hook(it);
  }
  if constexpr (NTILE % G::NS != 0) {
    asm volatile("" : "+s"(on));
#pragma nounroll
    for (int it = on; it > 0; --it) static_for<0, NTILE % G::NS>([&](auto Ss) { ktile<G, W, Ss.value>(p, ra, rb, acc); });
  }
}
template <class G, int NTILE, class Init, class Hook>
DI void gemm_loop(Pipe<G>& p, int lane, f32x4 (&acc)[G::MT][G::NT], int st0, Init init, Hook hook) {
  constexpr int ND = G::ND, H = G::H, DP = G::DEPTH;
  typename G::RA ra; typename G::RB rb;
  ra.init(p.lds, (p.wid >> 1) * G::MT, lane); rb.init(p.lds + G::BM * 128, (p.wid & 1) * G::NT, lane);
#pragma unroll
  for (int n = 0; n < G::NT; ++n) { const f32x4 v = init(n);
#pragma unroll
    for (int m = 0; m < G::MT; ++m) { acc[m][n] = v; pinacc(acc[m][n]); } }
  wait_vm<(DP - 2) * (ND + G::NX) + H>();
  bar();
  if (p.sig != nullptr && threadIdx.x == 0) __hip_atomic_store(p.sig, p.tag, __ATOMIC_RELAXED, __HIP_MEMORY_SCOPE_AGENT);
  static_for<0, G::NIA>([&](auto I) { ra.template rd<0, 0, I.value, 0>(); });
  static_for<0, G::NIB>([&](auto I) { rb.template rd<0, 0, I.value, 32>(); });
  wait_frags();
  STAMP(1, st0);
  asm volatile("s_nop 1");
  gemm_body<G, 0, NTILE>(p, ra, rb, acc, 1, hook);
  asm volatile("s_nop 7\n\ts_nop 7\n\ts_waitcnt vmcnt(0) lgkmcnt(0)" ::: "memory", ACLOB);
#pragma unroll
  for (int m = 0; m < G::MT; ++m)
#pragma unroll
    for (int n = 0; n < G::NT; ++n) pinacc(acc[m][n]);
  STAMP(1, st0 + 1);
}

DI void touch(const void* p) { asm volatile("global_load_dword a124, %0, off" ::"v"(p) : "memory"); }
template <int R, int OFF> DI void gld4_nt(const void* p) { asm volatile("global_load_dwordx4 a[%c1:%c2], %0, off offset:%c3 nt" ::"v"(p), "n"(R), "n"(R + 3), "n"(OFF) : "memory"); }
template <int R> DI void gld2(const void* p) { asm volatile("global_load_dwordx2 a[%c1:%c2], %0, off" ::"v"(p), "n"(R), "n"(R + 1) : "memory"); }
template <int R, int OFF> DI void bld4_dev(__amdgpu_buffer_rsrc_t r, unsigned voff, unsigned soff) { asm volatile("buffer_load_dwordx4 a[%c3:%c4], %0, %1, %2 offen offset:%c5 sc1" ::"v"(voff), "s"(r), "s"(soff), "n"(R), "n"(R + 3), "n"(OFF) : "memory"); }
template <int R> DI void flag_req(const unsigned* p) { asm volatile("global_load_dword a[%c1], %0, off sc1" ::"v"(p), "n"(R) : "memory"); }
template <int R> DI unsigned ard() { unsigned r; asm volatile("v_accvgpr_read_b32 %0, a[%c1]" : "=v"(r) : "n"(R)); return r; }
template <int R> DI u32x4 ard4() { return (u32x4){ard<R>(), ard<R + 1>(), ard<R + 2>(), ard<R + 3>()}; }
template <int R> DI u32x2 ard2() { return (u32x2){ard<R>(), ard<R + 1>()}; }
template <int CTRL> DI float dpp_add(float a) { return a + __builtin_bit_cast(float, __builtin_amdgcn_update_dpp(0, __builtin_bit_cast(int, a), CTRL, 0xf, 0xf, true)); }
template <int N> DI void wave_sum(float (&s)[N]) {
#pragma unroll
  for (int r = 0; r < N; ++r) s[r] = dpp_add<0xB1>(s[r]);
#pragma unroll
  for (int r = 0; r < N; ++r) s[r] = dpp_add<0x4E>(s[r]);
#pragma unroll
  for (int r = 0; r < N; ++r) s[r] = dpp_add<0x141>(s[r]);
#pragma unroll
  for (int r = 0; r < N; ++r) s[r] = dpp_add<0x140>(s[r]);
#pragma unroll
  for (int r = 0; r < N; ++r) { const auto p = __builtin_amdgcn_permlane16_swap(__float_as_uint(s[r]), __float_as_uint(s[r]), false, false); s[r] = __uint_as_float(p[0]) + __uint_as_float(p[1]); }
#pragma unroll
  for (int r = 0; r < N; ++r) { const auto p = __builtin_amdgcn_permlane32_swap(__float_as_uint(s[r]), __float_as_uint(s[r]), false, false); s[r] = __uint_as_float(p[0]) + __uint_as_float(p[1]); }
}

DI f32x2 sigm_mul(f32x2 x, f32x2 arg) {
  f32x2 e; e.x = __builtin_amdgcn_exp2f(arg.x); e.y = __builtin_amdgcn_exp2f(arg.y);
  const f32x2 d = e + 1.0f;
  f32x2 r; r.x = __builtin_amdgcn_rcpf(d.x); r.y = __builtin_amdgcn_rcpf(d.y);
  return x * r;
}
DI f32x2 gelu2(f32x2 x) {
  const float k0 = -2.0f * 0.7978845608028654f * 1.4426950408889634f, k1 = k0 * 0.044715f;
  return sigm_mul(x, x * ((x * x) * k1 + k0));
}
DI f32x2 silu2(f32x2 x) { return sigm_mul(x, x * -1.4426950408889634f); }
DI f32x4 gelu4(f32x4 t) { const f32x2 a = gelu2((f32x2){t[0], t[1]}), b = gelu2((f32x2){t[2], t[3]}); return (f32x4){a.x, a.y, b.x, b.y}; }
DI f32x4 bf4(u32x2 w) { return (f32x4){bf_lo(w[0]), bf_hi(w[0]), bf_lo(w[1]), bf_hi(w[1])}; }

#define GO_NS 4
#define GO_LAZY false
#define GO_RSP 11
#define DEVPOL 1
typedef Geo<16, 256, 128, 3, FRAG, ROWK, DEVPOL, 0, false, 1, false, 24> GAB;
#define YT1 3
#define YT2 11
#define ABT 4
#define PREP_DELAY 16
#define PREP_DELAY2 0
#define PREP_FIRST 0
#define EP1 1
#define EP2 5
#define ABS 8
#define YS0 5
#define YSN 5
#define WS2 1
#define HS0 3
#define HSN 8
typedef Geo<32, 256, 128, 3, ROWK, FRAG, 0, DEVPOL, true, WS2 == 0, false, 24> GY;
#define H_TOUCH (ABT < 4)
typedef Geo<16, 256, 224, 2, FRAG, NMAJ, DEVPOL, 0, true, WS2 == 1, false, 42> GH;
typedef Geo<56, 128, 128, GO_NS, FRAG, FRAG, DEVPOL, DEVPOL, true, 0, GO_LAZY, GO_RSP> GO;
constexpr int LDSM = GY::NS * GY::STAGE > GO::NS * GO::STAGE ? GY::NS * GY::STAGE : GO::NS * GO::STAGE;

DI u32x4 pack_pair(const f32x4& a, const f32x4& b) {
  const unsigned a0 = cvt_pk_bf16(a[0], a[1]), a1 = cvt_pk_bf16(a[2], a[3]), b0 = cvt_pk_bf16(b[0], b[1]), b1 = cvt_pk_bf16(b[2], b[3]);
  const auto r0 = __builtin_amdgcn_permlane16_swap(a0, b0, false, false);
  const auto r1 = __builtin_amdgcn_permlane16_swap(a1, b1, false, false);
  return (u32x4){r0[0], r1[0], r0[1], r1[1]};
}
DI u32x4 pack8(const f32x4& a, const f32x4& b) { return (u32x4){cvt_pk_bf16(a[0], a[1]), cvt_pk_bf16(a[2], a[3]), cvt_pk_bf16(b[0], b[1]), cvt_pk_bf16(b[2], b[3])}; }
DI unsigned hpos(int t, int b) { return b == 0 ? 2 * t : (b == 3 ? 2 * t + 1 : 32 + 5 * t + (b < 3 ? b - 1 : b - 2)); }
struct NoHook { DI void operator()(int) const {} };
struct ZeroInit { DI f32x4 operator()(int) const { return (f32x4){0.f, 0.f, 0.f, 0.f}; } };

DI void st_dev(__amdgpu_buffer_rsrc_t r, unsigned off, u32x4 v) { __builtin_amdgcn_raw_buffer_store_b128(v, r, off, 0, 16); }
DI void st_dev8(__amdgpu_buffer_rsrc_t r, unsigned off, u32x2 v) { __builtin_amdgcn_raw_buffer_store_b64(v, r, off, 0, 16); }
DI u32x4 ld_dev(__amdgpu_buffer_rsrc_t r, unsigned voff, unsigned soff) { return __builtin_amdgcn_raw_buffer_load_b128(r, voff, soff, 16); }
DI void signal(unsigned* f, unsigned tag) { if (threadIdx.x == 0) __hip_atomic_store(f, tag, __ATOMIC_RELAXED, __HIP_MEMORY_SCOPE_AGENT); }
DI void publish(unsigned* f, unsigned tag) { wait_vm<0>(); bar(); signal(f, tag); }
DI void poll(unsigned* f, int n, unsigned tag) {
  unsigned* q = f + ((int)(threadIdx.x & 63) < n ? (threadIdx.x & 63) : 0) * FS;
#pragma nounroll
  for (int i = 0; i < SPIN_CAP && !__all(__hip_atomic_load(q, __ATOMIC_RELAXED, __HIP_MEMORY_SCOPE_AGENT) == tag); ++i) __builtin_amdgcn_s_sleep(1);
}
DI void await(unsigned* f, int n, unsigned tag) {
  if (threadIdx.x < 64) poll(f, n, tag);
  asm volatile("" ::: "memory");
  bar();
}

__global__ __launch_bounds__(256, 1) void k_main(const u16* __restrict__ x, const u16* __restrict__ ws_re, const u16* __restrict__ ws_im, const u16* __restrict__ wd_re, const u16* __restrict__ wd_im,
                                                 const u16* __restrict__ w1, const u16* __restrict__ b1, const u16* __restrict__ w2, const u16* __restrict__ b2, unsigned char* ws, u16* __restrict__ out) {
  extern __shared__ __attribute__((aligned(16))) unsigned char shm[];
  {
    const char* pc; unsigned len;
    asm volatile("s_getpc_b64 %0\n.Lkm_pc:\n\ts_mov_b32 %1, .Lkm_endy-.Lkm_pc\n\ts_max_i32 %1, %1, .Lkm_endo-.Lkm_pc" : "=s"(pc), "=s"(len) :: "scc");
    asm volatile("buffer_load_dword a124, %0, %1, 0 offen" ::"v"((((blockIdx.x >> 3) & 1) * 256 + threadIdx.x) * 128), "s"(mkrsrc(pc, len)) : "memory", ACLOB);
  }
  const int wid = __builtin_amdgcn_readfirstlane(threadIdx.x >> 6), lane = threadIdx.x & 63, fr = lane & 15, fq = lane >> 4, wm = wid >> 1, wn = wid & 1;
  const int xcd = blockIdx.x & 7, slot = blockIdx.x >> 3, chain = slot >> 4, s4 = slot & 15;
  const int pm1 = (xcd & 1) * 4 + (s4 & 3), pn1 = (xcd >> 1) * 4 + (s4 >> 2);
  const int half = xcd & 1, pm = (xcd >> 2) * 4 + (s4 & 3), pn = ((xcd >> 1) & 1) * 4 + (s4 >> 2), tile = pm * 8 + pn;
  const unsigned ldsb = (unsigned)(size_t)(LAS unsigned char*)shm;
  const int lo = ((fq & 1) * 2 + (fq >> 1)) * 256 + fr * 16;
  unsigned* const flag0 = (unsigned*)(ws + WS_FLAG);
#define FLAG(i) (flag0 + (i) * FS)
  const __amdgpu_buffer_rsrc_t ry = mkrsrc(ws + WS_PY + (size_t)tile * 131072, 131072);
  STAMP(1, 0);
  const int kb = xcd * 16 + s4;
  unsigned tag;
  {
    const u16* px = x + (size_t)(blockIdx.x * 8 + wid * 2) * D_ + lane * 8;
    asm volatile("global_load_dword a16, %0, off sc1" ::"v"(FLAG(FL_E)) : "memory");
    gld4_nt<0, 0>(px); gld4_nt<4, 1024>(px); gld4_nt<8, 2048>(px); gld4_nt<12, 3072>(px); }
  auto layernorm = [&](auto NY) {
    wait_vm<NY.value>();
    const u32x4 raw[4] = {ard4<0>(), ard4<4>(), ard4<8>(), ard4<12>()};
    const unsigned ev = ard<16>();
    f32x2 v[2][8]; float sm[2], mean[2];
#pragma unroll
    for (int i = 0; i < 16; ++i) v[i >> 3][i & 7] = (f32x2){bf_lo(raw[i >> 2][i & 3]), bf_hi(raw[i >> 2][i & 3])};
#pragma unroll
    for (int r = 0; r < 2; ++r) { const f32x2 a = ((v[r][0] + v[r][1]) + (v[r][2] + v[r][3])) + ((v[r][4] + v[r][5]) + (v[r][6] + v[r][7])); sm[r] = a.x + a.y; }
    wave_sum(sm);
#pragma unroll
    for (int r = 0; r < 2; ++r) { mean[r] = sm[r] * (1.0f / D_); f32x2 a = {0.f, 0.f};
#pragma unroll
      for (int e = 0; e < 8; ++e) { const f32x2 dd = v[r][e] - mean[r]; a += dd * dd; }
      sm[r] = a.x + a.y; }
    wave_sum(sm);
    LAS unsigned char* tl = (LAS unsigned char*)shm;
#pragma unroll
    for (int r = 0; r < 2; ++r) { const float rden = 1.0f / rbf(sqrtf(rbf(rbf(sm[r] * (1.0f / D_)) + 1e-5f))), mb = rbf(mean[r]);
#pragma unroll
      for (int j = 0; j < 2; ++j) { u32x4 o;
#pragma unroll
        for (int e = 0; e < 4; ++e) { const f32x2 d = v[r][4 * j + e] - mb; const unsigned db = cvt_pk_bf16(d.x, d.y); const f32x2 q = (f32x2){bf_lo(db), bf_hi(db)} * rden; o[e] = cvt_pk_bf16(q.x, q.y); }
        *(LAS u32x4*)(tl + (j * 64 + lane) * 128 + ((wid * 2 + r + lane) & 7) * 16) = o; } }
    tag = __builtin_amdgcn_readfirstlane(ev) + 1u;
    wait_lgkm0(); bar();
    STAMP(1, 14);
    const __amdgpu_buffer_rsrc_t rx = mkrsrc(ws + WS_XA + (size_t)(blockIdx.x >> 1) * 32768, 32768);
#pragma unroll
    for (int j = 0; j < 4; ++j) { const int ch = (wid * 4 + j) * 8 + (lane >> 3);
      st_dev(rx, ch * 256 + (blockIdx.x & 1) * 128 + (lane & 7) * 16, *(const LAS u32x4*)(tl + ch * 128 + ((lane + ch) & 7) * 16)); }
    publish(FLAG(FL_X + blockIdx.x), tag);
    STAMP(1, 10);
  };
  if (chain == 0) {
    const u16* wsm = half ? ws_im : ws_re;
    const u16* wshare = wsm + (size_t)(pm * 256 + pn * 32) * S_;
    f32x4 acc[8][4];
    {
      Pipe<GAB> p; p.lds = ldsb; p.wid = wid;
      p.B.init_rowk(pn1 < 8 ? wd_re : wd_im, D_ * D_ * 2, D_, (pn1 & 7) * 128, 0, wid, lane);
      p.prologue<2, false>();
      layernorm(std::integral_constant<int, 8>{});
      __builtin_amdgcn_s_sleep(PREP_DELAY);
      {
        const int t = (s4 * 8 + xcd) * 256 + threadIdx.x;
        const int c = (s4 & 3) * 256 + threadIdx.x;
        if (t < PREP_FIRST) touch((const char*)w1 + (size_t)(t + 7168) * 128);
        __builtin_amdgcn_s_sleep(PREP_DELAY2);
        touch((const char*)w1 + (size_t)t * 128);
        touch(t < 24576 ? (const char*)w1 + (size_t)(t + 32768) * 128 : (c < 512 ? (const char*)b1 + (c % 56) * 128 : (const char*)b2 + (c & 15) * 128));
        touch((const char*)(t < 16384 ? wd_re : wd_im) + (size_t)(t & 16383) * 128);
        const __amdgpu_buffer_rsrc_t rs = mkrsrc(w2 + (size_t)(kb < 112 ? kb : 0) * 32 * D_, kb < 112 ? 32 * D_ * 2 : 0);
        asm volatile("s_nop 4" ::: "memory");
        static_for<0, 16>([&](auto I) { constexpr int i = I.value;
          const int g = ((i >> 1) & 3) | ((wid & 1) << 2);
          asm volatile("s_add_u32 m0, %0, %1\n\ts_nop 0" ::"s"(ldsb + (1 + wm) * GAB::STAGE + wn * 16384), "n"(i * 1024) : "memory", "scc");
          dma16<0, 2>((lane ^ (g << 1)) * 16, rs, wid * 16384 + i * 1024); });
      }
      wait_vm<0>(); bar();
      if (kb < 112) {
        const int q = fr >> 2, gk = q | ((fq & 1) << 2);
        const __amdgpu_buffer_rsrc_t rw = mkrsrc(ws + WS_W2, F_ * D_ * 2);
        LAS unsigned char* src = (LAS unsigned char*)shm + (1 + (fq >> 1)) * GAB::STAGE + (8 * (fq & 1) + q) * 2048 + wid * 512 + 8 * (lane & 3);
        const unsigned kp = hpos(kb / 7, kb % 7);
#pragma unroll
        for (int n = 0; n < 16; ++n) {
          const bf16x4 l4 = __builtin_amdgcn_ds_read_tr16_b64_v4i16((LAS bf16x4*)(src + 32 * (n ^ gk)));
          const bf16x4 h4 = __builtin_amdgcn_ds_read_tr16_b64_v4i16((LAS bf16x4*)(src + 32 * (n ^ gk) + 8192));
          const u32x2 a = __builtin_bit_cast(u32x2, l4), b = __builtin_bit_cast(u32x2, h4);
          st_dev(rw, (unsigned)((wid * 16 + n) * 112 + kp) * 1024u + lane * 16, (u32x4){a[0], a[1], b[0], b[1]});
        }
        wait_lgkm0();
      }
      STAMP(1, 4);
      p.xl.rs = mkrsrc(wshare + (size_t)wid * 8 * S_, 32768); p.xl.step = 16384; p.xl.soff = 0u - (unsigned)(kb % ABS) * 16384u; p.xl.voff = fq * 4096 + fr * 128;
      p.A.init_frag(ws + WS_XA, S_ * D_ * 2, 32, pm1 * 256, 0, wid, lane);
      await(FLAG(FL_X + pm1 * 32), 32, tag);
      p.prologue<1, true>();
      p.sig = FLAG(FL_W2 + kb); p.tag = tag;
      gemm_loop<GAB, 16>(p, lane, acc, 1, ZeroInit(), NoHook());
      const __amdgpu_buffer_rsrc_t ro = mkrsrc(ws + WS_AB, S_ * 2 * D_ * 2);
      const unsigned ob = (unsigned)((pn1 * 8 + wn * 4) * 64 + pm1 * 8 + wm * 4) * 1024u + lo;
#pragma unroll
      for (int n = 0; n < 4; ++n)
#pragma unroll
        for (int m = 0; m < 8; m += 2) st_dev(ro, ob + (n * 64 + m / 2) * 1024, pack_pair(acc[m][n], acc[m + 1][n]));
      publish(FLAG(FL_AB + pn1 * 8 + pm1), tag);
      STAMP(1, 3);
    }
    {
      Pipe<GY> p; p.lds = ldsb; p.wid = wid;
      p.xl.rs = mkrsrc(wshare + (size_t)wid * 8 * S_, 32768); p.xl.step = 16384; p.xl.soff = 0u - (unsigned)(YS0 + kb % YSN) * 16384u; p.xl.voff = fq * 4096 + fr * 128 + 2048;
      p.A.init_rowk(wsm, S_ * S_ * 2, S_, pm * 256, 0, wid, lane);
      p.B.init_frag(ws + WS_AB, S_ * 2 * D_ * 2, 64, half * D_ + pn * 128, 0, wid, lane);
      p.prologue<1, false>();
      await(FLAG(FL_AB + (half * 8 + pn) * 8), 8, tag);
      STAMP(1, 5);
      p.prologue<2, true>();
      gemm_loop<GY, 32>(p, lane, acc, 6, ZeroInit(), NoHook());
#pragma unroll
      for (int i = 0; i < 16; ++i) st_dev(ry, lane * 16 + half * 65536 + wm * 32768 + (((i >> 3) * 2 + wn) * 8 + (i & 7)) * 1024, pack8(acc[i >> 1][(i & 1) * 2], acc[i >> 1][(i & 1) * 2 + 1]));
      asm volatile(".Lkm_endy:" ::: "memory");
      publish(FLAG(FL_Y + tile * 2 + half), tag);
      STAMP(1, 8);
    }
  } else {
    Pipe<GO> po; po.lds = ldsb; po.wid = wid;
    const size_t xo = (size_t)(pm * 256 + half * 128 + wm * 64 + (lane >> 3)) * D_ + pn * 128 + wn * 64 + (lane & 7) * 8;
    {
      const int c0 = pn1 * 224;
      Pipe<GH> p; p.lds = ldsb; p.wid = wid;
      p.xl.rs = mkrsrc((half ? ws_im : ws_re) + (size_t)(pm * 256 + pn * 32 + wid * 8) * S_, 32768); p.xl.step = 16384; p.xl.soff = 0u - (unsigned)(HS0 + kb % HSN) * 16384u; p.xl.voff = fq * 4096 + fr * 128 + 2048;
      static_for<0, 7>([&](auto N) { constexpr int n = N.value; gld2<32 + 2 * n>(b1 + c0 + (n < 6 ? wn * 96 + n * 16 : 192 + wn * 16) + fq * 4); });
      p.B.init_nmaj(w1, D_ * F_ * 2, F_, c0, 0, wid, lane);
      p.prologue<2, false>();
      layernorm(std::integral_constant<int, 21>{});
      p.A.init_frag(ws + WS_XA, S_ * D_ * 2, 32, pm1 * 256, 0, wid, lane);
      await(FLAG(FL_X + pm1 * 32), 32, tag);
      STAMP(1, 11);
      p.prologue<1, true>();
      f32x4 acc[8][7];
      const u32x2 bias[7] = {ard2<32>(), ard2<34>(), ard2<36>(), ard2<38>(), ard2<40>(), ard2<42>(), ard2<44>()};
      gemm_loop<GH, 16>(p, lane, acc, 1, [&](int n) { return bf4(bias[n]); }, NoHook());
      flag_req<125>(FLAG(FL_W2 + lane)); flag_req<126>(FLAG(FL_W2 + 64 + lane));
      const __amdgpu_buffer_rsrc_t ro = mkrsrc(ws + WS_H, S_ * F_ * 2);
      const unsigned ob = (unsigned)((pm1 * 16 + wm * 8) * 112) * 1024u, o1 = ob + (2 * pn1 + wn) * 1024 + lo, o2 = ob + (32 + 5 * pn1 + 2 * wn) * 1024 + lo,
                     os = ob + (32 + 5 * pn1 + 4) * 1024 + (wn * 2 + (fq >> 1)) * 256 + fr * 16 + (fq & 1) * 8;
#pragma unroll
      for (int m = 0; m < 8; ++m) st_dev(ro, o1 + m * 112 * 1024, pack_pair(gelu4(acc[m][0]), gelu4(acc[m][1])));
      asm volatile("" ::: "memory");
#pragma unroll
      for (int m = 0; m < 8; ++m) {
        st_dev(ro, o2 + m * 112 * 1024, pack_pair(gelu4(acc[m][2]), gelu4(acc[m][3])));
        st_dev(ro, o2 + m * 112 * 1024 + 1024, pack_pair(gelu4(acc[m][4]), gelu4(acc[m][5])));
        const f32x4 g = gelu4(acc[m][6]); st_dev8(ro, os + m * 112 * 1024, (u32x2){cvt_pk_bf16(g[0], g[1]), cvt_pk_bf16(g[2], g[3])});
        if (m == EP1) {
          wait_vm<3 * (EP1 + 1)>(); bar(); signal(FLAG(FL_H + pm1 * 32 + pn1), tag);
          if (!__all(ard<125>() == tag && ard<126>() == tag)) { poll(FLAG(FL_W2), 64, tag); poll(FLAG(FL_W2 + 64), 64, tag); }
          po.B.init_frag(ws + WS_W2, F_ * D_ * 2, 112, pn * 128, 0, wid, lane);
          po.prologue<2, false>();
          po.A.init_frag(ws + WS_H, S_ * F_ * 2, 112, pm * 256 + half * 128, 0, wid, lane);
        }
        if (m == EP2 - 1) flag_req<125>(FLAG(FL_H + pm * 32 + (lane & 15)));
        if (m == EP2) {
          wait_vm<3>();
          if (!__all(ard<125>() == tag)) poll(FLAG(FL_H + pm * 32), 16, tag);
          asm volatile("" ::: "memory");
          STAMP(1, 5);
          po.prologue<1, true>();
        }
      }
      STAMP(1, 3);
    }
    f32x4 acc[4][4];
    unsigned* fh2 = FLAG(FL_H + pm * 32 + 16);
    unsigned* fy = FLAG(FL_Y + tile * 2);
    auto yreq = [&]() {
      static_for<0, 16>([&](auto I) { constexpr int i = I.value;
        bld4_dev<128 + 4 * i, (i & 3) * 1024>(ry, lane * 16, (i >> 3) * 65536 + half * 32768 + wid * 8192 + (i & 4) * 1024); });
    };
    gemm_loop<GO, 56>(po, lane, acc, 6, ZeroInit(), [&](int it) {
      if (it == 14) signal(FLAG(FL_H + pm1 * 32 + 16 + pn1), tag);
      if (it == 13) flag_req<125>(fh2 + (lane & 15) * FS);
      if (it == 12) { if (!__all(ard<125>() == tag)) poll(fh2, 16, tag); asm volatile("" ::: "memory"); }
      if (it == 2) { flag_req<125>(fy + (lane & 1) * FS); static_for<0, 4>([&](auto N) { gld2<80 + 2 * N.value>(b2 + pn * 128 + wn * 64 + N.value * 16 + fq * 4); }); }
    });
    if (!__all(ard<125>() == tag)) poll(fy, 2, tag);
    asm volatile("" ::: "memory");
    yreq();
    static_for<0, 8>([&](auto J) { constexpr int j = J.value; gld4_nt<16 + 4 * (j & 3) + 32 * (j >> 2), 0>(x + xo + (size_t)j * 8 * D_); });
    STAMP(1, 8);
    const u32x2 bias[4] = {ard2<80>(), ard2<82>(), ard2<84>(), ard2<86>()};
#pragma unroll
    for (int i = 0; i < 16; ++i) { const f32x4 t = acc[i >> 2][i & 3] + bf4(bias[i & 3]);
      const f32x2 a = silu2((f32x2){t[0], t[1]}), b = silu2((f32x2){t[2], t[3]}); acc[i >> 2][i & 3] = (f32x4){a.x, a.y, b.x, b.y}; pinacc(acc[i >> 2][i & 3]); }
    wait_vm<0>(); bar();
    const u32x4 xr[8] = {ard4<16>(), ard4<20>(), ard4<24>(), ard4<28>(), ard4<48>(), ard4<52>(), ard4<56>(), ard4<60>()};
    u32x4 yr[8], yi[8];
    static_for<0, 8>([&](auto I) { yr[I.value] = ard4<128 + 4 * I.value>(); yi[I.value] = ard4<160 + 4 * I.value>(); });
    LAS unsigned char* tl = (LAS unsigned char*)shm + wid * (64 * 144);
#pragma unroll
    for (int i = 0; i < 8; ++i) {
      unsigned r[4];
#pragma unroll
      for (int e = 0; e < 4; ++e) { const f32x4 g = acc[i >> 1][(i & 1) * 2 + (e >> 1)];
        r[e] = cvt_pk_bf16((bf_lo(yr[i][e]) - bf_lo(yi[i][e])) * g[(e & 1) * 2], (bf_hi(yr[i][e]) - bf_hi(yi[i][e])) * g[(e & 1) * 2 + 1]); }
      LAS unsigned char* q = tl + ((i >> 1) * 16 + fr) * 144 + (i & 1) * 64 + fq * 8;
      *(LAS u32x2*)q = (u32x2){r[0], r[1]}; *(LAS u32x2*)(q + 32) = (u32x2){r[2], r[3]};
    }
    wait_lgkm0();
#pragma unroll
    for (int j = 0; j < 8; ++j) {
      const u32x4 r = *(const LAS u32x4*)(tl + (j * 8 + (lane >> 3)) * 144 + (lane & 7) * 16);
      u32x4 o;
#pragma unroll
      for (int e = 0; e < 4; ++e) o[e] = cvt_pk_bf16(bf_lo(xr[j][e]) + bf_lo(r[e]), bf_hi(xr[j][e]) + bf_hi(r[e]));
      __builtin_nontemporal_store(o, (u32x4*)(out + xo + (size_t)j * 8 * D_));
    }
    asm volatile(".Lkm_endo:" ::: "memory");
    if (blockIdx.x == 255) {
      poll(FLAG(FL_X + wid * 64), 64, tag);
      asm volatile("" ::: "memory"); bar();
      signal(FLAG(FL_E), tag);
    }
    STAMP(1, 9);
  }
}

extern "C" void kernel_launch(void* const* d_in, const int* in_sizes, int n_in, void* d_out, int out_size, void* d_ws, size_t ws_size, hipStream_t stream) {
  const u16* x = (const u16*)d_in[0]; const u16* ws_re = (const u16*)d_in[1]; const u16* ws_im = (const u16*)d_in[2];
  const u16* wd_re = (const u16*)d_in[3]; const u16* wd_im = (const u16*)d_in[4];
  const u16* w1 = (const u16*)d_in[6]; const u16* b1 = (const u16*)d_in[7]; const u16* w2 = (const u16*)d_in[8]; const u16* b2 = (const u16*)d_in[9];
  unsigned char* ws = (unsigned char*)d_ws;
  (void)hipFuncSetAttribute((const void*)k_main, hipFuncAttributeMaxDynamicSharedMemorySize, LDSM);
  k_main<<<256, 256, LDSM, stream>>>(x, ws_re, ws_im, wd_re, wd_im, w1, b1, w2, b2, ws, (u16*)d_out);
}
```

```cpp
#include <hip/hip_runtime.h>
#include <hip/hip_bf16.h>
#include <type_traits>
#include <utility>

#define LAS __attribute__((address_space(3)))
#define DI __device__ __forceinline__
typedef unsigned short u16;
typedef short bf16x4 __attribute__((ext_vector_type(4)));
typedef float f32x4 __attribute__((ext_vector_type(4)));
typedef float f32x2 __attribute__((ext_vector_type(2)));
typedef unsigned u32x4 __attribute__((ext_vector_type(4)));
typedef unsigned u32x2 __attribute__((ext_vector_type(2)));

constexpr int S_ = 2048, D_ = 1024, F_ = 3584;
constexpr size_t WS_FLAG = 0;
#define FS_ 16
constexpr int FS = FS_, FL_E = 0, FL_X = 32, FL_AB = FL_X + 256, FL_H = FL_AB + 128, FL_W2 = FL_H + 256, FL_Y = FL_W2 + 128;
constexpr size_t WS_XA = 262144;
constexpr size_t WS_AB = WS_XA + (size_t)S_ * D_ * 2;
constexpr size_t WS_H = WS_AB + (size_t)S_ * 2 * D_ * 2;
constexpr size_t WS_W2 = WS_H + (size_t)S_ * F_ * 2;
constexpr size_t WS_PY = WS_W2 + (size_t)F_ * D_ * 2;
constexpr unsigned OOB = 0x40000000u;
constexpr int SPIN_CAP = 1 << 20;

template <int N> DI void wait_vm() { asm volatile("s_waitcnt vmcnt(%0)" ::"n"(N) : "memory"); }
DI void wait_lgkm0() { asm volatile("s_waitcnt lgkmcnt(0)" ::: "memory"); }
template <int N> DI void wait_lgkm() { asm volatile("s_waitcnt lgkmcnt(%0)" ::"n"(N > 15 ? 15 : N) : "memory"); }
DI void bar() { asm volatile("s_barrier" ::: "memory"); }
DI unsigned cvt_pk_bf16(float lo, float hi) {
  typedef __bf16 bf16x2 __attribute__((ext_vector_type(2)));
  f32x2 v = {lo, hi}; bf16x2 b = __builtin_convertvector(v, bf16x2); return __builtin_bit_cast(unsigned, b);
}
DI float bf_lo(unsigned w) { return __uint_as_float(w << 16); }
DI float bf_hi(unsigned w) { return __uint_as_float(w & 0xffff0000u); }
DI float rbf(float v) { return bf_lo(cvt_pk_bf16(v, v)); }
template <int I, int N, class F> DI void static_for(F&& f) { if constexpr (I < N) { f(std::integral_constant<int, I>{}); static_for<I + 1, N>(f); } }

#define ACLOB "a0","a1","a2","a3","a4","a5","a6","a7","a8","a9","a10","a11","a12","a13","a14","a15","a16","a17","a18","a19","a20","a21","a22","a23","a24","a25","a26","a27","a28","a29","a30","a31","a32","a33","a34","a35","a36","a37","a38","a39","a40","a41","a42","a43","a44","a45","a46","a47","a48","a49","a50","a51","a52","a53","a54","a55","a56","a57","a58","a59","a60","a61","a62","a63","a64","a65","a66","a67","a68","a69","a70","a71","a72","a73","a74","a75","a76","a77","a78","a79","a80","a81","a82","a83","a84","a85","a86","a87","a88","a89","a90","a91","a92","a93","a94","a95","a96","a97","a98","a99","a100","a101","a102","a103","a104","a105","a106","a107","a108","a109","a110","a111","a112","a113","a114","a115","a116","a117","a118","a119","a120","a121","a122","a123","a124","a125","a126","a127","a128","a129","a130","a131","a132","a133","a134","a135","a136","a137","a138","a139","a140","a141","a142","a143","a144","a145","a146","a147","a148","a149","a150","a151","a152","a153","a154","a155","a156","a157","a158","a159","a160","a161","a162","a163","a164","a165","a166","a167","a168","a169","a170","a171","a172","a173","a174","a175","a176","a177","a178","a179","a180","a181","a182","a183","a184","a185","a186","a187","a188","a189","a190","a191"
template <int RA, int RB> DI void mfma16(f32x4& acc) { asm volatile("v_mfma_f32_16x16x32_bf16 %0, a[%c1:%c2], a[%c3:%c4], %0" : "+v"(acc) : "n"(RA), "n"(RA + 3), "n"(RB), "n"(RB + 3)); }
template <int RD, int OFF> DI void ds_r128(unsigned addr) { asm volatile("ds_read_b128 a[%c1:%c2], %0 offset:%c3" ::"v"(addr), "n"(RD), "n"(RD + 3), "n"(OFF) : "memory"); }
template <int RD, int OFF> DI void ds_rtr(unsigned addr) { asm volatile("ds_read_b64_tr_b16 a[%c1:%c2], %0 offset:%c3" ::"v"(addr), "n"(RD), "n"(RD + 1), "n"(OFF) : "memory"); }
DI void wait_frags() { asm volatile("s_waitcnt lgkmcnt(0)" ::: "memory", ACLOB); }
template <int OFF, int POL> DI void dma16(int voff, __amdgpu_buffer_rsrc_t r, unsigned soff) {
  if constexpr (POL == 0) asm volatile("buffer_load_dwordx4 %0, %1, %2 offen offset:%3 lds" ::"v"(voff), "s"(r), "s"(soff), "n"(OFF) : "memory");
  if constexpr (POL == 1) asm volatile("buffer_load_dwordx4 %0, %1, %2 offen offset:%3 sc1 lds" ::"v"(voff), "s"(r), "s"(soff), "n"(OFF) : "memory");
  if constexpr (POL == 2) asm volatile("buffer_load_dwordx4 %0, %1, %2 offen offset:%3 nt lds" ::"v"(voff), "s"(r), "s"(soff), "n"(OFF) : "memory");
}
DI void pinacc(f32x4& v) { asm volatile("" : "+v"(v)); }
template <int C> DI int s_eq(int v) { int r; asm volatile("s_cmp_eq_u32 %1, %2\n\ts_cselect_b32 %0, 1, 0" : "=s"(r) : "s"(v), "n"(C) : "scc"); return r; }
DI __amdgpu_buffer_rsrc_t mkrsrc(const void* p, unsigned bytes) { return __builtin_amdgcn_make_buffer_rsrc((void*)p, 0, bytes, 0x00020000); }

#define STAMP(k, i) do {} while (0)
#define STAGGER 0
#define SNAKE 1
#define LN_PASS0 0
enum { ROWK = 0, FRAG = 1, NMAJ = 2 };

template <int KIND, int R, int POL> struct Stream {
  static constexpr bool ODD = KIND == NMAJ && R == 224;
  static constexpr int P = R / 32, NG = (P + 3) / 4, NV = ODD ? 7 : (KIND == FRAG ? 2 : 4);
  __amdgpu_buffer_rsrc_t rs;
  int voff[NV];
  unsigned s0[NG], ks;
  unsigned ml[NG];
  static DI int gpiece(int g, int wid) { if constexpr (ODD) return wid * 7 + 4 * g; else return 16 * g + 4 * wid; }
  DI void init_rowk(const void* p, unsigned bytes, int ld, int r0, int k0, int wid, int lane) {
    rs = mkrsrc(p, bytes);
#pragma unroll
    for (int j = 0; j < 4; ++j) voff[j] = ((lane >> 3) + 8 * j) * ld * 2 + (((lane & 7) ^ ((4 * (j & 1) + (lane >> 4)) & 7)) * 16) - j * 1024;
#pragma unroll
    for (int g = 0; g < NG; ++g) s0[g] = (unsigned)((r0 + 8 * gpiece(g, wid)) * ld + k0) * 2u;
    ks = 128u;
  }
  DI void init_frag(const void* p, unsigned bytes, int kbn, int r0, int k0, int wid, int lane) {
    rs = mkrsrc(p, bytes);
    voff[0] = lane * 16; voff[1] = lane * 16 + kbn * 1024 - 2048;
#pragma unroll
    for (int g = 0; g < NG; ++g) s0[g] = (unsigned)((r0 / 16 + gpiece(g, wid) / 2) * kbn + k0 / 32) * 1024u;
    ks = 2048u;
  }
  DI void init_nmaj(const void* p, unsigned bytes, int ld, int n0, int k0, int wid, int lane) {
    rs = mkrsrc(p, bytes);
    if constexpr (ODD) {
#pragma unroll
      for (int j = 0; j < 7; ++j) { const int L = (wid * 7 + j) * 64 + lane, kk = L / 28, pos = L % 28;
        voff[j] = kk * ld * 2 + (pos ^ (((kk >> 3) & 1) << 1)) * 16 - (j & 3) * 1024; }
      s0[0] = s0[1] = (unsigned)(k0 * ld + n0) * 2u;
    } else {
      static_assert(R == 128 || ODD, "");
#pragma unroll
      for (int j = 0; j < 4; ++j) { const int kk = 4 * (4 * wid + j) + (lane >> 4), g = (kk & 3) | (((kk >> 3) & 1) << 2);
        voff[j] = (4 * j + (lane >> 4)) * ld * 2 + (((lane & 15) ^ (g << 1)) * 16) - j * 1024; }
      s0[0] = (unsigned)((k0 + 16 * wid) * ld + n0) * 2u;
    }
    ks = (unsigned)(64 * ld) * 2u;
  }
  DI void init_lds(unsigned base, int wid) {
#pragma unroll
    for (int g = 0; g < NG; ++g) ml[g] = base + (unsigned)gpiece(g, wid) * 1024u;
  }
  template <int I, int SO> DI void m0() const { asm volatile("s_add_u32 m0, %0, %1" ::"s"(ml[I / 4]), "n"(SO) : "memory", "scc"); }
  template <int I> DI void issue() const {
    constexpr int g = I / 4, j = I % 4, vi = ODD ? I : (KIND == FRAG ? (j >> 1) : j);
    dma16<j * 1024, POL>(voff[vi], rs, s0[g]);
  }
  unsigned se[NG];
  DI void init_end(int nt) {
#pragma unroll
    for (int g = 0; g < NG; ++g) se[g] = s0[g] + nt * ks;
  }
  template <int I> DI void advance() { asm volatile("s_add_u32 %0, %0, %1\n\ts_cmp_eq_u32 %0, %2\n\ts_cselect_b32 %0, 2.0, %0" : "+s"(s0[I / 4]) : "s"(ks), "s"(se[I / 4]) : "scc"); }
};

template <int KIND, int R, int T, int NS, int STAGE> struct Reader {
  static constexpr int NA = KIND == FRAG ? 1 : (KIND == ROWK ? 2 : 3);
  static constexpr int SPAN = KIND == NMAJ ? 64 * R * 2 : T * 2048, SPW = (65536 - SPAN) / STAGE + 1, NW = (NS + SPW - 1) / SPW;
  unsigned a[NW][NA];
  DI void init(unsigned base, int t0, int lane) {
    const int fr = lane & 15, fq = lane >> 4;
    if constexpr (KIND == ROWK) { a[0][0] = base + (t0 * 16 + fr) * 128 + ((fq ^ (fr >> 1)) * 16); a[0][1] = base + (t0 * 16 + fr) * 128 + (((4 + fq) ^ (fr >> 1)) * 16); }
    else if constexpr (KIND == FRAG) a[0][0] = base + t0 * 2048 + lane * 16;
    else { static_assert(KIND != NMAJ || R == 224, "");
      const int q = fr >> 2, p = lane & 3, wn = t0 / 7; const unsigned b = base + (8 * fq + q) * 448 + 8 * p;
      a[0][0] = b + 32 * ((6 * wn) ^ (fq & 1)); a[0][1] = b + 32 * ((6 * wn + 1) ^ (fq & 1)); a[0][2] = b + 32 * ((12 + wn) ^ (fq & 1)); }
#pragma unroll
    for (int w = 1; w < NW; ++w)
#pragma unroll
      for (int n = 0; n < NA; ++n) { a[w][n] = a[0][n] + w * SPW * STAGE; asm volatile("" : "+v"(a[w][n])); }
  }
  template <int ST, int KS, int I, int RB> DI void rd() const {
    constexpr int w = ST / SPW, so = (ST % SPW) * STAGE;
    if constexpr (KIND == ROWK) ds_r128<RB + 4 * I, so + I * 2048>(a[w][KS]);
    else if constexpr (KIND == FRAG) ds_r128<RB + 4 * I, so + I * 2048 + KS * 1024>(a[w][0]);
    else { constexpr int n = I / 2, h = I % 2; ds_rtr<RB + 4 * n + 2 * h, so + (32 * KS + 4 * h) * (R * 2) + (n < 6 ? 32 * (n & ~1) : 0)>(a[w][n < 6 ? (n & 1) : 2]); }
  }
};

template <int NTILE_, int BM_, int BN_, int NS_, int KA_, int KB_, int PLA_, int PLB_, bool TR_, int NX_, bool LAZY_, int RSP_> struct Geo {
  static constexpr bool LAZY = LAZY_; static constexpr int NTILE = NTILE_, NX = NX_, PLA = PLA_, PLB = PLB_;
  static constexpr int BM = BM_, BN = BN_, NS = NS_, KA = KA_, KB = KB_, MT = BM / 32, NT = BN / 32, NM = MT * NT;
  static constexpr bool TR = TR_;
  static constexpr int PA = BM / 32, PB = BN / 32, ND = PA + PB, STAGE = (BM + BN) * 128;
  static constexpr int DEPTH = LAZY ? NS - 1 : NS;
  static constexpr int H = (DEPTH == 2) ? ND : ND / 2;
  static constexpr int NIA = KA == NMAJ ? 2 * MT : MT, NIB = KB == NMAJ ? 2 * NT : NT, NR = NIA + NIB, RSPAN = RSP_;
  static constexpr int rgap(int r) { return (r * RSPAN) / NR; }
  static constexpr int rbefore(int i) { int c = 0; for (int r = 0; r < NR; ++r) c += rgap(r) < i; return c; }
  typedef Reader<KA, BM, MT, NS, STAGE> RA; typedef Reader<KB, BN, NT, NS, STAGE> RB;
};
struct Side { __amdgpu_buffer_rsrc_t rs; unsigned voff, step, soff; };
template <class G> struct Pipe {
  Stream<G::KA, G::BM, G::PLA> A; Stream<G::KB, G::BN, G::PLB> B;
  Side xl, x2;
  unsigned ls = 0;
  unsigned* sig = nullptr; unsigned tag = 0;
  unsigned lds; int wid;
  DI void side() {
    if constexpr (G::NX > 0) asm volatile("buffer_load_dword a124, %0, %1, %2 offen" ::"v"(xl.voff), "s"(xl.rs), "s"(xl.soff) : "memory");
    if constexpr (G::NX > 1) asm volatile("buffer_load_dword a124, %0, %1, %2 offen" ::"v"(x2.voff), "s"(x2.rs), "s"(x2.soff) : "memory"); }
  DI void side_adv() {
    if constexpr (G::NX > 0) asm volatile("s_add_u32 %0, %0, %1" : "+s"(xl.soff) : "s"(xl.step) : "scc");
    if constexpr (G::NX > 1) asm volatile("s_add_u32 %0, %0, %1" : "+s"(x2.soff) : "s"(x2.step) : "scc"); }
  static constexpr bool gstart(int i) { return i < G::PA ? (i % 4 == 0) : ((i - G::PA) % 4 == 0); }
  static constexpr bool gend(int i) { return i == G::PA - 1 || i == G::ND - 1 || gstart(i + 1); }
  template <int I, int ST> DI void m0() const { if constexpr (I < G::PA) A.template m0<I, ST * G::STAGE>(); else B.template m0<I - G::PA, ST * G::STAGE>(); }
  template <int I> DI void issue() const { if constexpr (I < G::PA) A.template issue<I>(); else B.template issue<I - G::PA>(); }
  template <int I> DI void advance() { if constexpr (I < G::PA) A.template advance<I>(); else B.template advance<I - G::PA>(); }
  template <int MASK, bool LAST> DI void prologue() {
    if constexpr (MASK & 1) { A.init_lds(lds, wid); A.init_end(G::NTILE); }
    if constexpr (MASK & 2) { B.init_lds(lds + G::BM * 128, wid); B.init_end(G::NTILE); }
    asm volatile("s_nop 4" ::: "memory", ACLOB);
    static_for<0, G::DEPTH>([&](auto Ss) {
      if constexpr (LAST && Ss.value >= 2) { side(); side_adv(); }
      static_for<0, (Ss.value == G::DEPTH - 1 ? G::H : G::ND)>([&](auto Dd) { constexpr int d = Dd.value;
        if constexpr ((MASK >> (d < G::PA ? 0 : 1)) & 1) {
          if constexpr (gstart(d)) { m0<d, Ss.value>(); asm volatile("s_nop 0"); }
          issue<d>();
          if constexpr (gend(d)) advance<d>(); }
        else if constexpr (LAST && Ss.value >= 1) asm volatile("buffer_load_dword a124, off, %0, %1" ::"s"(A.rs), "s"(OOB) : "memory"); }); });
    if constexpr (LAST && G::H < G::ND) if constexpr (!gstart(G::H)) m0<G::H, G::DEPTH - 1>();
  }
};

constexpr int dgap(int d, int ndp, int nm, int w) { return 1 + (d * (nm - 2) + w * (nm - 2) / 4) / ndp; }
constexpr int dat(int i, int ndp, int nm, int w) { for (int d = 0; d < ndp; ++d) if (dgap(d, ndp, nm, w) == i) return d; return -1; }
template <class G, int W, int CUR, int RST, int RKS, int D0, int D1, int DST, int I>
DI void gap(Pipe<G>& p, const typename G::RA& ra, const typename G::RB& rb, f32x4 (&acc)[G::MT][G::NT]) {
  constexpr int CA = 64 * CUR, CB = CA + 32, NA = 64 * (CUR ^ 1), NB = NA + 32;
  constexpr int NM = G::NM, NIB = G::NIB, NR = G::NR, NDP = D1 - D0, m = I / G::NT, n0 = I % G::NT, n = (SNAKE && (m & 1)) ? G::NT - 1 - n0 : n0;
  constexpr int d = dat(I, NDP, NM, W), dp = dat(I - 1, NDP, NM, W);
  if constexpr (n0 == 0 && m % 2 == 0 && (G::LAZY || CUR == 0)) wait_lgkm<(NR - 1 - (NIB + m + 1)) + G::rbefore(I)>();
  if constexpr (d >= 0) if constexpr (Pipe<G>::gstart(D0 + d)) p.template m0<D0 + d, DST>();
  if constexpr (G::TR) mfma16<CB + 4 * n, CA + 4 * m>(acc[m][n]); else mfma16<CA + 4 * m, CB + 4 * n>(acc[m][n]);
  if constexpr (I == 0 && CUR == (G::DEPTH == 2 ? 0 : 1)) p.side();
  if constexpr (I == NM - 1 && CUR == (G::DEPTH == 2 ? 0 : 1)) p.side_adv();
  if constexpr (dp >= 0) if constexpr (Pipe<G>::gend(D0 + dp)) p.template advance<D0 + dp>();
  if constexpr (d >= 0) p.template issue<D0 + d>();
  static_for<G::rbefore(I), G::rbefore(I + 1)>([&](auto Rr) { constexpr int r = Rr.value;
    if constexpr (r == 0) ra.template rd<RST, RKS, 0, NA>();
    else if constexpr (r <= NIB) rb.template rd<RST, RKS, r - 1, NB>();
    else ra.template rd<RST, RKS, r - NIB, NA>(); });
}
template <class G, int W, int CUR, int RST, int RKS, int D0, int D1, int DST, int... Is>
DI void phase_(Pipe<G>& p, const typename G::RA& ra, const typename G::RB& rb, f32x4 (&acc)[G::MT][G::NT], std::integer_sequence<int, Is...>) {
  (gap<G, W, CUR, RST, RKS, D0, D1, DST, Is>(p, ra, rb, acc), ...);
}
template <class G, int W, int ST>
DI void ktile(Pipe<G>& p, const typename G::RA& ra, const typename G::RB& rb, f32x4 (&acc)[G::MT][G::NT]) {
  constexpr int NS = G::NS, ND = G::ND, H = G::H, DP = G::DEPTH;
  phase_<G, W, 0, ST, 1, H, ND, (ST + DP - 1) % NS>(p, ra, rb, acc, std::make_integer_sequence<int, G::NM>{});
  if constexpr (G::LAZY) asm volatile("s_waitcnt vmcnt(%0)\n\ts_barrier" ::"n"((DP - 2) * (ND + G::NX)) : "memory", ACLOB);
  else asm volatile("s_waitcnt vmcnt(%0) lgkmcnt(0)\n\ts_barrier" ::"n"(DP == 2 ? G::NX : (DP - 2) * (ND + G::NX)) : "memory", ACLOB);
  phase_<G, W, 1, (ST + 1) % NS, 0, 0, H, (ST + DP) % NS>(p, ra, rb, acc, std::make_integer_sequence<int, G::NM>{});
}
template <class G, int W, int NTILE, class Hook>
DI void gemm_body(Pipe<G>& p, const typename G::RA& ra, const typename G::RB& rb, f32x4 (&acc)[G::MT][G::NT], int on, Hook hook) {
#pragma nounroll
  for (int it = on * (NTILE / G::NS); it > 0; --it) { static_for<0, G::NS>([&](auto Ss) { ktile<G, W, Ss.value>(p, ra, rb, acc); });
    hook(it);
  }
  if constexpr (NTILE % G::NS != 0) {
    asm volatile("" : "+s"(on));
#pragma nounroll
    for (int it = on; it > 0; --it) static_for<0, NTILE % G::NS>([&](auto Ss) { ktile<G, W, Ss.value>(p, ra, rb, acc); });
  }
}
template <class G, int NTILE, class Init, class Hook>
DI void gemm_loop(Pipe<G>& p, int lane, f32x4 (&acc)[G::MT][G::NT], int st0, Init init, Hook hook) {
  constexpr int ND = G::ND, H = G::H, DP = G::DEPTH;
  typename G::RA ra; typename G::RB rb;
  ra.init(p.lds, (p.wid >> 1) * G::MT, lane); rb.init(p.lds + G::BM * 128, (p.wid & 1) * G::NT, lane);
#pragma unroll
  for (int n = 0; n < G::NT; ++n) { const f32x4 v = init(n);
#pragma unroll
    for (int m = 0; m < G::MT; ++m) { acc[m][n] = v; pinacc(acc[m][n]); } }
  wait_vm<(DP - 2) * (ND + G::NX) + H>();
  bar();
  if (p.sig != nullptr && threadIdx.x == 0) __hip_atomic_store(p.sig, p.tag, __ATOMIC_RELAXED, __HIP_MEMORY_SCOPE_AGENT);
  static_for<0, G::NIA>([&](auto I) { ra.template rd<0, 0, I.value, 0>(); });
  static_for<0, G::NIB>([&](auto I) { rb.template rd<0, 0, I.value, 32>(); });
  wait_frags();
  STAMP(1, st0);
  asm volatile("s_nop 1");
  gemm_body<G, 0, NTILE>(p, ra, rb, acc, 1, hook);
  asm volatile("s_nop 7\n\ts_nop 7\n\ts_waitcnt vmcnt(0) lgkmcnt(0)" ::: "memory", ACLOB);
#pragma unroll
  for (int m = 0; m < G::MT; ++m)
#pragma unroll
    for (int n = 0; n < G::NT; ++n) pinacc(acc[m][n]);
  STAMP(1, st0 + 1);
}

DI void touch(const void* p) { asm volatile("global_load_dword a124, %0, off" ::"v"(p) : "memory"); }
template <int R, int OFF> DI void gld4_nt(const void* p) { asm volatile("global_load_dwordx4 a[%c1:%c2], %0, off offset:%c3 nt" ::"v"(p), "n"(R), "n"(R + 3), "n"(OFF) : "memory"); }
template <int R> DI void gld2(const void* p) { asm volatile("global_load_dwordx2 a[%c1:%c2], %0, off" ::"v"(p), "n"(R), "n"(R + 1) : "memory"); }
template <int R, int OFF> DI void bld4_dev(__amdgpu_buffer_rsrc_t r, unsigned voff, unsigned soff) { asm volatile("buffer_load_dwordx4 a[%c3:%c4], %0, %1, %2 offen offset:%c5 sc1" ::"v"(voff), "s"(r), "s"(soff), "n"(R), "n"(R + 3), "n"(OFF) : "memory"); }
template <int R> DI void flag_req(const unsigned* p) { asm volatile("global_load_dword a[%c1], %0, off sc1" ::"v"(p), "n"(R) : "memory"); }
template <int R> DI unsigned ard() { unsigned r; asm volatile("v_accvgpr_read_b32 %0, a[%c1]" : "=v"(r) : "n"(R)); return r; }
template <int R> DI u32x4 ard4() { return (u32x4){ard<R>(), ard<R + 1>(), ard<R + 2>(), ard<R + 3>()}; }
template <int R> DI u32x2 ard2() { return (u32x2){ard<R>(), ard<R + 1>()}; }
template <int CTRL> DI float dpp_add(float a) { return a + __builtin_bit_cast(float, __builtin_amdgcn_update_dpp(0, __builtin_bit_cast(int, a), CTRL, 0xf, 0xf, true)); }
template <int N> DI void wave_sum(float (&s)[N]) {
#pragma unroll
  for (int r = 0; r < N; ++r) s[r] = dpp_add<0xB1>(s[r]);
#pragma unroll
  for (int r = 0; r < N; ++r) s[r] = dpp_add<0x4E>(s[r]);
#pragma unroll
  for (int r = 0; r < N; ++r) s[r] = dpp_add<0x141>(s[r]);
#pragma unroll
  for (int r = 0; r < N; ++r) s[r] = dpp_add<0x140>(s[r]);
#pragma unroll
  for (int r = 0; r < N; ++r) { const auto p = __builtin_amdgcn_permlane16_swap(__float_as_uint(s[r]), __float_as_uint(s[r]), false, false); s[r] = __uint_as_float(p[0]) + __uint_as_float(p[1]); }
#pragma unroll
  for (int r = 0; r < N; ++r) { const auto p = __builtin_amdgcn_permlane32_swap(__float_as_uint(s[r]), __float_as_uint(s[r]), false, false); s[r] = __uint_as_float(p[0]) + __uint_as_float(p[1]); }
}

DI f32x2 sigm_mul(f32x2 x, f32x2 arg) {
  f32x2 e; e.x = __builtin_amdgcn_exp2f(arg.x); e.y = __builtin_amdgcn_exp2f(arg.y);
  const f32x2 d = e + 1.0f;
  f32x2 r; r.x = __builtin_amdgcn_rcpf(d.x); r.y = __builtin_amdgcn_rcpf(d.y);
  return x * r;
}
DI f32x2 gelu2(f32x2 x) {
  const float k0 = -2.0f * 0.7978845608028654f * 1.4426950408889634f, k1 = k0 * 0.044715f;
  return sigm_mul(x, x * ((x * x) * k1 + k0));
}
DI f32x2 silu2(f32x2 x) { return sigm_mul(x, x * -1.4426950408889634f); }
DI f32x4 gelu4(f32x4 t) { const f32x2 a = gelu2((f32x2){t[0], t[1]}), b = gelu2((f32x2){t[2], t[3]}); return (f32x4){a.x, a.y, b.x, b.y}; }
DI f32x4 bf4(u32x2 w) { return (f32x4){bf_lo(w[0]), bf_hi(w[0]), bf_lo(w[1]), bf_hi(w[1])}; }

#define GO_NS 4
#define GO_LAZY false
#define GO_RSP 11
#define DEVPOL 1
typedef Geo<16, 256, 128, 3, FRAG, ROWK, DEVPOL, 0, false, 1, false, 24> GAB;
#define YT1 3
#define YT2 11
#define ABT 4
#define PREP_DELAY 16
#define PREP_DELAY2 0
#define PREP_FIRST 0
#define EP1 1
#define EP2 5
#define TLBW 2
#define ABS 8
#define YS0 5
#define YSN 5
#define WS2 1
#define HS0 3
#define HSN 8
typedef Geo<32, 256, 128, 3, ROWK, FRAG, 0, DEVPOL, true, WS2 == 0, false, 24> GY;
#define H_TOUCH (ABT < 4)
typedef Geo<16, 256, 224, 2, FRAG, NMAJ, DEVPOL, 0, true, WS2 == 1, false, 42> GH;
typedef Geo<56, 128, 128, GO_NS, FRAG, FRAG, DEVPOL, DEVPOL, true, 0, GO_LAZY, GO_RSP> GO;
constexpr int LDSM = GY::NS * GY::STAGE > GO::NS * GO::STAGE ? GY::NS * GY::STAGE : GO::NS * GO::STAGE;

DI u32x4 pack_pair(const f32x4& a, const f32x4& b) {
  const unsigned a0 = cvt_pk_bf16(a[0], a[1]), a1 = cvt_pk_bf16(a[2], a[3]), b0 = cvt_pk_bf16(b[0], b[1]), b1 = cvt_pk_bf16(b[2], b[3]);
  const auto r0 = __builtin_amdgcn_permlane16_swap(a0, b0, false, false);
  const auto r1 = __builtin_amdgcn_permlane16_swap(a1, b1, false, false);
  return (u32x4){r0[0], r1[0], r0[1], r1[1]};
}
DI u32x4 pack8(const f32x4& a, const f32x4& b) { return (u32x4){cvt_pk_bf16(a[0], a[1]), cvt_pk_bf16(a[2], a[3]), cvt_pk_bf16(b[0], b[1]), cvt_pk_bf16(b[2], b[3])}; }
DI unsigned hpos(int t, int b) { return b == 0 ? 2 * t : (b == 3 ? 2 * t + 1 : 32 + 5 * t + (b < 3 ? b - 1 : b - 2)); }
struct NoHook { DI void operator()(int) const {} };
struct ZeroInit { DI f32x4 operator()(int) const { return (f32x4){0.f, 0.f, 0.f, 0.f}; } };

DI void st_dev(__amdgpu_buffer_rsrc_t r, unsigned off, u32x4 v) { __builtin_amdgcn_raw_buffer_store_b128(v, r, off, 0, 16); }
DI void st_dev8(__amdgpu_buffer_rsrc_t r, unsigned off, u32x2 v) { __builtin_amdgcn_raw_buffer_store_b64(v, r, off, 0, 16); }
DI u32x4 ld_dev(__amdgpu_buffer_rsrc_t r, unsigned voff, unsigned soff) { return __builtin_amdgcn_raw_buffer_load_b128(r, voff, soff, 16); }
DI void signal(unsigned* f, unsigned tag) { if (threadIdx.x == 0) __hip_atomic_store(f, tag, __ATOMIC_RELAXED, __HIP_MEMORY_SCOPE_AGENT); }
DI void publish(unsigned* f, unsigned tag) { wait_vm<0>(); bar(); signal(f, tag); }
DI void poll(unsigned* f, int n, unsigned tag) {
  unsigned* q = f + ((int)(threadIdx.x & 63) < n ? (threadIdx.x & 63) : 0) * FS;
#pragma nounroll
  for (int i = 0; i < SPIN_CAP && !__all(__hip_atomic_load(q, __ATOMIC_RELAXED, __HIP_MEMORY_SCOPE_AGENT) == tag); ++i) __builtin_amdgcn_s_sleep(1);
}
DI void await(unsigned* f, int n, unsigned tag) {
  if (threadIdx.x < 64) poll(f, n, tag);
  asm volatile("" ::: "memory");
  bar();
}

__global__ __launch_bounds__(256, 1) void k_main(const u16* __restrict__ x, const u16* __restrict__ ws_re, const u16* __restrict__ ws_im, const u16* __restrict__ wd_re, const u16* __restrict__ wd_im,
                                                 const u16* __restrict__ w1, const u16* __restrict__ b1, const u16* __restrict__ w2, const u16* __restrict__ b2, unsigned char* ws, u16* __restrict__ out) {
  extern __shared__ __attribute__((aligned(16))) unsigned char shm[];
  {
    const char* pc; unsigned len;
    asm volatile("s_getpc_b64 %0\n.Lkm_pc:\n\ts_mov_b32 %1, .Lkm_endy-.Lkm_pc\n\ts_max_i32 %1, %1, .Lkm_endo-.Lkm_pc" : "=s"(pc), "=s"(len) :: "scc");
    asm volatile("buffer_load_dword a124, %0, %1, 0 offen" ::"v"((((blockIdx.x >> 3) & 1) * 256 + threadIdx.x) * 128), "s"(mkrsrc(pc, len)) : "memory", ACLOB);
  }
  const int wid = __builtin_amdgcn_readfirstlane(threadIdx.x >> 6), lane = threadIdx.x & 63, fr = lane & 15, fq = lane >> 4, wm = wid >> 1, wn = wid & 1;
  const int xcd = blockIdx.x & 7, slot = blockIdx.x >> 3, chain = slot >> 4, s4 = slot & 15;
  const int pm1 = (xcd & 1) * 4 + (s4 & 3), pn1 = (xcd >> 1) * 4 + (s4 >> 2);
  const int half = xcd & 1, pm = (xcd >> 2) * 4 + (s4 & 3), pn = ((xcd >> 1) & 1) * 4 + (s4 >> 2), tile = pm * 8 + pn;
  const unsigned ldsb = (unsigned)(size_t)(LAS unsigned char*)shm;
  const int lo = ((fq & 1) * 2 + (fq >> 1)) * 256 + fr * 16;
  unsigned* const flag0 = (unsigned*)(ws + WS_FLAG);
#define FLAG(i) (flag0 + (i) * FS)
  const __amdgpu_buffer_rsrc_t ry = mkrsrc(ws + WS_PY + (size_t)tile * 131072, 131072);
  STAMP(1, 0);
  const int kb = xcd * 16 + s4;
  if (wid == TLBW - 1) {
    int q = slot * 64 + lane; const char* p = (const char*)ws;
    constexpr int WSE = (int)((WS_PY + (size_t)64 * 131072) >> 16);
    if (q >= WSE) { q -= WSE; p = (const char*)ws_re;
      if (q >= 128) { q -= 128; p = (const char*)ws_im;
        if (q >= 128) { q -= 128; p = (const char*)w1;
          if (q >= 112) { q -= 112; p = (const char*)w2;
            if (q >= 112) { q -= 112; p = (const char*)out;
              if (q >= 64) { q -= 64; p = (const char*)x;
                if (q >= 64) { q -= 64; p = (const char*)wd_re;
                  if (q >= 32) { q = (q - 32) & 31; p = (const char*)wd_im; } } } } } } } }
    touch(p + (size_t)q * 65536);
  }
  unsigned tag;
  {
    const u16* px = x + (size_t)(blockIdx.x * 8 + wid * 2) * D_ + lane * 8;
    asm volatile("global_load_dword a16, %0, off sc1" ::"v"(FLAG(FL_E)) : "memory");
    gld4_nt<0, 0>(px); gld4_nt<4, 1024>(px); gld4_nt<8, 2048>(px); gld4_nt<12, 3072>(px); }
  auto layernorm = [&](auto NY) {
    wait_vm<NY.value>();
    const u32x4 raw[4] = {ard4<0>(), ard4<4>(), ard4<8>(), ard4<12>()};
    const unsigned ev = ard<16>();
    f32x2 v[2][8]; float sm[2], mean[2];
#pragma unroll
    for (int i = 0; i < 16; ++i) v[i >> 3][i & 7] = (f32x2){bf_lo(raw[i >> 2][i & 3]), bf_hi(raw[i >> 2][i & 3])};
#pragma unroll
    for (int r = 0; r < 2; ++r) { const f32x2 a = ((v[r][0] + v[r][1]) + (v[r][2] + v[r][3])) + ((v[r][4] + v[r][5]) + (v[r][6] + v[r][7])); sm[r] = a.x + a.y; }
    wave_sum(sm);
#pragma unroll
    for (int r = 0; r < 2; ++r) { mean[r] = sm[r] * (1.0f / D_); f32x2 a = {0.f, 0.f};
#pragma unroll
      for (int e = 0; e < 8; ++e) { const f32x2 dd = v[r][e] - mean[r]; a += dd * dd; }
      sm[r] = a.x + a.y; }
    wave_sum(sm);
    LAS unsigned char* tl = (LAS unsigned char*)shm;
#pragma unroll
    for (int r = 0; r < 2; ++r) { const float rden = 1.0f / rbf(sqrtf(rbf(rbf(sm[r] * (1.0f / D_)) + 1e-5f))), mb = rbf(mean[r]);
#pragma unroll
      for (int j = 0; j < 2; ++j) { u32x4 o;
#pragma unroll
        for (int e = 0; e < 4; ++e) { const f32x2 d = v[r][4 * j + e] - mb; const unsigned db = cvt_pk_bf16(d.x, d.y); const f32x2 q = (f32x2){bf_lo(db), bf_hi(db)} * rden; o[e] = cvt_pk_bf16(q.x, q.y); }
        *(LAS u32x4*)(tl + (j * 64 + lane) * 128 + ((wid * 2 + r + lane) & 7) * 16) = o; } }
    tag = __builtin_amdgcn_readfirstlane(ev) + 1u;
    wait_lgkm0(); bar();
    STAMP(1, 14);
    const __amdgpu_buffer_rsrc_t rx = mkrsrc(ws + WS_XA + (size_t)(blockIdx.x >> 1) * 32768, 32768);
#pragma unroll
    for (int j = 0; j < 4; ++j) { const int ch = (wid * 4 + j) * 8 + (lane >> 3);
      st_dev(rx, ch * 256 + (blockIdx.x & 1) * 128 + (lane & 7) * 16, *(const LAS u32x4*)(tl + ch * 128 + ((lane + ch) & 7) * 16)); }
    publish(FLAG(FL_X + blockIdx.x), tag);
    STAMP(1, 10);
  };
  if (chain == 0) {
    const u16* wsm = half ? ws_im : ws_re;
    const u16* wshare = wsm + (size_t)(pm * 256 + pn * 32) * S_;
    f32x4 acc[8][4];
    {
      Pipe<GAB> p; p.lds = ldsb; p.wid = wid;
      p.B.init_rowk(pn1 < 8 ? wd_re : wd_im, D_ * D_ * 2, D_, (pn1 & 7) * 128, 0, wid, lane);
      p.prologue<2, false>();
      layernorm(std::integral_constant<int, 8>{});
      __builtin_amdgcn_s_sleep(PREP_DELAY);
      {
        const int t = (s4 * 8 + xcd) * 256 + threadIdx.x;
        const int c = (s4 & 3) * 256 + threadIdx.x;
        if (t < PREP_FIRST) touch((const char*)w1 + (size_t)(t + 7168) * 128);
        __builtin_amdgcn_s_sleep(PREP_DELAY2);
        touch((const char*)w1 + (size_t)t * 128);
        touch(t < 24576 ? (const char*)w1 + (size_t)(t + 32768) * 128 : (c < 512 ? (const char*)b1 + (c % 56) * 128 : (const char*)b2 + (c & 15) * 128));
        touch((const char*)(t < 16384 ? wd_re : wd_im) + (size_t)(t & 16383) * 128);
        const __amdgpu_buffer_rsrc_t rs = mkrsrc(w2 + (size_t)(kb < 112 ? kb : 0) * 32 * D_, kb < 112 ? 32 * D_ * 2 : 0);
        asm volatile("s_nop 4" ::: "memory");
        static_for<0, 16>([&](auto I) { constexpr int i = I.value;
          const int g = ((i >> 1) & 3) | ((wid & 1) << 2);
          asm volatile("s_add_u32 m0, %0, %1\n\ts_nop 0" ::"s"(ldsb + (1 + wm) * GAB::STAGE + wn * 16384), "n"(i * 1024) : "memory", "scc");
          dma16<0, 2>((lane ^ (g << 1)) * 16, rs, wid * 16384 + i * 1024); });
      }
      wait_vm<0>(); bar();
      if (kb < 112) {
        const int q = fr >> 2, gk = q | ((fq & 1) << 2);
        const __amdgpu_buffer_rsrc_t rw = mkrsrc(ws + WS_W2, F_ * D_ * 2);
        LAS unsigned char* src = (LAS unsigned char*)shm + (1 + (fq >> 1)) * GAB::STAGE + (8 * (fq & 1) + q) * 2048 + wid * 512 + 8 * (lane & 3);
        const unsigned kp = hpos(kb / 7, kb % 7);
#pragma unroll
        for (int n = 0; n < 16; ++n) {
          const bf16x4 l4 = __builtin_amdgcn_ds_read_tr16_b64_v4i16((LAS bf16x4*)(src + 32 * (n ^ gk)));
          const bf16x4 h4 = __builtin_amdgcn_ds_read_tr16_b64_v4i16((LAS bf16x4*)(src + 32 * (n ^ gk) + 8192));
          const u32x2 a = __builtin_bit_cast(u32x2, l4), b = __builtin_bit_cast(u32x2, h4);
          st_dev(rw, (unsigned)((wid * 16 + n) * 112 + kp) * 1024u + lane * 16, (u32x4){a[0], a[1], b[0], b[1]});
        }
        wait_lgkm0();
      }
      STAMP(1, 4);
      p.xl.rs = mkrsrc(wshare + (size_t)wid * 8 * S_, 32768); p.xl.step = 16384; p.xl.soff = 0u - (unsigned)(kb % ABS) * 16384u; p.xl.voff = fq * 4096 + fr * 128;
      p.A.init_frag(ws + WS_XA, S_ * D_ * 2, 32, pm1 * 256, 0, wid, lane);
      await(FLAG(FL_X + pm1 * 32), 32, tag);
      p.prologue<1, true>();
      p.sig = FLAG(FL_W2 + kb); p.tag = tag;
      gemm_loop<GAB, 16>(p, lane, acc, 1, ZeroInit(), NoHook());
      const __amdgpu_buffer_rsrc_t ro = mkrsrc(ws + WS_AB, S_ * 2 * D_ * 2);
      const unsigned ob = (unsigned)((pn1 * 8 + wn * 4) * 64 + pm1 * 8 + wm * 4) * 1024u + lo;
#pragma unroll
      for (int n = 0; n < 4; ++n)
#pragma unroll
        for (int m = 0; m < 8; m += 2) st_dev(ro, ob + (n * 64 + m / 2) * 1024, pack_pair(acc[m][n], acc[m + 1][n]));
      publish(FLAG(FL_AB + pn1 * 8 + pm1), tag);
      STAMP(1, 3);
    }
    {
      Pipe<GY> p; p.lds = ldsb; p.wid = wid;
      p.xl.rs = mkrsrc(wshare + (size_t)wid * 8 * S_, 32768); p.xl.step = 16384; p.xl.soff = 0u - (unsigned)(YS0 + kb % YSN) * 16384u; p.xl.voff = fq * 4096 + fr * 128 + 2048;
      p.A.init_rowk(wsm, S_ * S_ * 2, S_, pm * 256, 0, wid, lane);
      p.B.init_frag(ws + WS_AB, S_ * 2 * D_ * 2, 64, half * D_ + pn * 128, 0, wid, lane);
      p.prologue<1, false>();
      await(FLAG(FL_AB + (half * 8 + pn) * 8), 8, tag);
      STAMP(1, 5);
      p.prologue<2, true>();
      gemm_loop<GY, 32>(p, lane, acc, 6, ZeroInit(), NoHook());
#pragma unroll
      for (int i = 0; i < 16; ++i) st_dev(ry, lane * 16 + half * 65536 + wm * 32768 + (((i >> 3) * 2 + wn) * 8 + (i & 7)) * 1024, pack8(acc[i >> 1][(i & 1) * 2], acc[i >> 1][(i & 1) * 2 + 1]));
      asm volatile(".Lkm_endy:" ::: "memory");
      publish(FLAG(FL_Y + tile * 2 + half), tag);
      STAMP(1, 8);
    }
  } else {
    Pipe<GO> po; po.lds = ldsb; po.wid = wid;
    const size_t xo = (size_t)(pm * 256 + half * 128 + wm * 64 + (lane >> 3)) * D_ + pn * 128 + wn * 64 + (lane & 7) * 8;
    {
      const int c0 = pn1 * 224;
      Pipe<GH> p; p.lds = ldsb; p.wid = wid;
      p.xl.rs = mkrsrc((half ? ws_im : ws_re) + (size_t)(pm * 256 + pn * 32 + wid * 8) * S_, 32768); p.xl.step = 16384; p.xl.soff = 0u - (unsigned)(HS0 + kb % HSN) * 16384u; p.xl.voff = fq * 4096 + fr * 128 + 2048;
      static_for<0, 7>([&](auto N) { constexpr int n = N.value; gld2<32 + 2 * n>(b1 + c0 + (n < 6 ? wn * 96 + n * 16 : 192 + wn * 16) + fq * 4); });
      p.B.init_nmaj(w1, D_ * F_ * 2, F_, c0, 0, wid, lane);
      p.prologue<2, false>();
      layernorm(std::integral_constant<int, 21>{});
      p.A.init_frag(ws + WS_XA, S_ * D_ * 2, 32, pm1 * 256, 0, wid, lane);
      await(FLAG(FL_X + pm1 * 32), 32, tag);
      STAMP(1, 11);
      p.prologue<1, true>();
      f32x4 acc[8][7];
      const u32x2 bias[7] = {ard2<32>(), ard2<34>(), ard2<36>(), ard2<38>(), ard2<40>(), ard2<42>(), ard2<44>()};
      gemm_loop<GH, 16>(p, lane, acc, 1, [&](int n) { return bf4(bias[n]); }, NoHook());
      flag_req<125>(FLAG(FL_W2 + lane)); flag_req<126>(FLAG(FL_W2 + 64 + lane));
      const __amdgpu_buffer_rsrc_t ro = mkrsrc(ws + WS_H, S_ * F_ * 2);
      const unsigned ob = (unsigned)((pm1 * 16 + wm * 8) * 112) * 1024u, o1 = ob + (2 * pn1 + wn) * 1024 + lo, o2 = ob + (32 + 5 * pn1 + 2 * wn) * 1024 + lo,
                     os = ob + (32 + 5 * pn1 + 4) * 1024 + (wn * 2 + (fq >> 1)) * 256 + fr * 16 + (fq & 1) * 8;
#pragma unroll
      for (int m = 0; m < 8; ++m) st_dev(ro, o1 + m * 112 * 1024, pack_pair(gelu4(acc[m][0]), gelu4(acc[m][1])));
      asm volatile("" ::: "memory");
#pragma unroll
      for (int m = 0; m < 8; ++m) {
        st_dev(ro, o2 + m * 112 * 1024, pack_pair(gelu4(acc[m][2]), gelu4(acc[m][3])));
        st_dev(ro, o2 + m * 112 * 1024 + 1024, pack_pair(gelu4(acc[m][4]), gelu4(acc[m][5])));
        const f32x4 g = gelu4(acc[m][6]); st_dev8(ro, os + m * 112 * 1024, (u32x2){cvt_pk_bf16(g[0], g[1]), cvt_pk_bf16(g[2], g[3])});
        if (m == EP1) {
          wait_vm<3 * (EP1 + 1)>(); bar(); signal(FLAG(FL_H + pm1 * 32 + pn1), tag);
          if (!__all(ard<125>() == tag && ard<126>() == tag)) { poll(FLAG(FL_W2), 64, tag); poll(FLAG(FL_W2 + 64), 64, tag); }
          po.B.init_frag(ws + WS_W2, F_ * D_ * 2, 112, pn * 128, 0, wid, lane);
          po.prologue<2, false>();
          po.A.init_frag(ws + WS_H, S_ * F_ * 2, 112, pm * 256 + half * 128, 0, wid, lane);
        }
        if (m == EP2 - 1) flag_req<125>(FLAG(FL_H + pm * 32 + (lane & 15)));
        if (m == EP2) {
          wait_vm<3>();
          if (!__all(ard<125>() == tag)) poll(FLAG(FL_H + pm * 32), 16, tag);
          asm volatile("" ::: "memory");
          STAMP(1, 5);
          po.prologue<1, true>();
        }
      }
      STAMP(1, 3);
    }
    f32x4 acc[4][4];
    unsigned* fh2 = FLAG(FL_H + pm * 32 + 16);
    unsigned* fy = FLAG(FL_Y + tile * 2);
    auto yreq = [&]() {
      static_for<0, 16>([&](auto I) { constexpr int i = I.value;
        bld4_dev<128 + 4 * i, (i & 3) * 1024>(ry, lane * 16, (i >> 3) * 65536 + half * 32768 + wid * 8192 + (i & 4) * 1024); });
    };
    gemm_loop<GO, 56>(po, lane, acc, 6, ZeroInit(), [&](int it) {
      if (it == 14) signal(FLAG(FL_H + pm1 * 32 + 16 + pn1), tag);
      if (it == 13) flag_req<125>(fh2 + (lane & 15) * FS);
      if (it == 12) { if (!__all(ard<125>() == tag)) poll(fh2, 16, tag); asm volatile("" ::: "memory"); }
      if (it == 2) { flag_req<125>(fy + (lane & 1) * FS); static_for<0, 4>([&](auto N) { gld2<80 + 2 * N.value>(b2 + pn * 128 + wn * 64 + N.value * 16 + fq * 4); }); }
    });
    if (!__all(ard<125>() == tag)) poll(fy, 2, tag);
    asm volatile("" ::: "memory");
    yreq();
    static_for<0, 8>([&](auto J) { constexpr int j = J.value; gld4_nt<16 + 4 * (j & 3) + 32 * (j >> 2), 0>(x + xo + (size_t)j * 8 * D_); });
    STAMP(1, 8);
    const u32x2 bias[4] = {ard2<80>(), ard2<82>(), ard2<84>(), ard2<86>()};
#pragma unroll
    for (int i = 0; i < 16; ++i) { const f32x4 t = acc[i >> 2][i & 3] + bf4(bias[i & 3]);
      const f32x2 a = silu2((f32x2){t[0], t[1]}), b = silu2((f32x2){t[2], t[3]}); acc[i >> 2][i & 3] = (f32x4){a.x, a.y, b.x, b.y}; pinacc(acc[i >> 2][i & 3]); }
    wait_vm<0>(); bar();
    const u32x4 xr[8] = {ard4<16>(), ard4<20>(), ard4<24>(), ard4<28>(), ard4<48>(), ard4<52>(), ard4<56>(), ard4<60>()};
    u32x4 yr[8], yi[8];
    static_for<0, 8>([&](auto I) { yr[I.value] = ard4<128 + 4 * I.value>(); yi[I.value] = ard4<160 + 4 * I.value>(); });
    LAS unsigned char* tl = (LAS unsigned char*)shm + wid * (64 * 144);
#pragma unroll
    for (int i = 0; i < 8; ++i) {
      unsigned r[4];
#pragma unroll
      for (int e = 0; e < 4; ++e) { const f32x4 g = acc[i >> 1][(i & 1) * 2 + (e >> 1)];
        r[e] = cvt_pk_bf16((bf_lo(yr[i][e]) - bf_lo(yi[i][e])) * g[(e & 1) * 2], (bf_hi(yr[i][e]) - bf_hi(yi[i][e])) * g[(e & 1) * 2 + 1]); }
      LAS unsigned char* q = tl + ((i >> 1) * 16 + fr) * 144 + (i & 1) * 64 + fq * 8;
      *(LAS u32x2*)q = (u32x2){r[0], r[1]}; *(LAS u32x2*)(q + 32) = (u32x2){r[2], r[3]};
    }
    wait_lgkm0();
#pragma unroll
    for (int j = 0; j < 8; ++j) {
      const u32x4 r = *(const LAS u32x4*)(tl + (j * 8 + (lane >> 3)) * 144 + (lane & 7) * 16);
      u32x4 o;
#pragma unroll
      for (int e = 0; e < 4; ++e) o[e] = cvt_pk_bf16(bf_lo(xr[j][e]) + bf_lo(r[e]), bf_hi(xr[j][e]) + bf_hi(r[e]));
      __builtin_nontemporal_store(o, (u32x4*)(out + xo + (size_t)j * 8 * D_));
    }
    asm volatile(".Lkm_endo:" ::: "memory");
    if (blockIdx.x == 255) {
      poll(FLAG(FL_X + wid * 64), 64, tag);
      asm volatile("" ::: "memory"); bar();
      signal(FLAG(FL_E), tag);
    }
    STAMP(1, 9);
  }
}

extern "C" void kernel_launch(void* const* d_in, const int* in_sizes, int n_in, void* d_out, int out_size, void* d_ws, size_t ws_size, hipStream_t stream) {
  const u16* x = (const u16*)d_in[0]; const u16* ws_re = (const u16*)d_in[1]; const u16* ws_im = (const u16*)d_in[2];
  const u16* wd_re = (const u16*)d_in[3]; const u16* wd_im = (const u16*)d_in[4];
  const u16* w1 = (const u16*)d_in[6]; const u16* b1 = (const u16*)d_in[7]; const u16* w2 = (const u16*)d_in[8]; const u16* b2 = (const u16*)d_in[9];
  unsigned char* ws = (unsigned char*)d_ws;
  (void)hipFuncSetAttribute((const void*)k_main, hipFuncAttributeMaxDynamicSharedMemorySize, LDSM);
  k_main<<<256, 256, LDSM, stream>>>(x, ws_re, ws_im, wd_re, wd_im, w1, b1, w2, b2, ws, (u16*)d_out);
}
```

```cpp
#include <hip/hip_runtime.h>
#include <hip/hip_bf16.h>
#include <type_traits>
#include <utility>

#define LAS __attribute__((address_space(3)))
#define DI __device__ __forceinline__
typedef unsigned short u16;
typedef short bf16x4 __attribute__((ext_vector_type(4)));
typedef float f32x4 __attribute__((ext_vector_type(4)));
typedef float f32x2 __attribute__((ext_vector_type(2)));
typedef unsigned u32x4 __attribute__((ext_vector_type(4)));
typedef unsigned u32x2 __attribute__((ext_vector_type(2)));

constexpr int S_ = 2048, D_ = 1024, F_ = 3584;
constexpr size_t WS_FLAG = 0;
#define FS_ 16
constexpr int FS = FS_, FL_E = 0, FL_X = 32, FL_AB = FL_X + 256, FL_H = FL_AB + 128, FL_W2 = FL_H + 256, FL_Y = FL_W2 + 128;
constexpr size_t WS_XA = 262144;
constexpr size_t WS_AB = WS_XA + (size_t)S_ * D_ * 2;
constexpr size_t WS_H = WS_AB + (size_t)S_ * 2 * D_ * 2;
constexpr size_t WS_W2 = WS_H + (size_t)S_ * F_ * 2;
constexpr size_t WS_PY = WS_W2 + (size_t)F_ * D_ * 2;
constexpr unsigned OOB = 0x40000000u;
constexpr int SPIN_CAP = 1 << 20;

template <int N> DI void wait_vm() { asm volatile("s_waitcnt vmcnt(%0)" ::"n"(N) : "memory"); }
DI void wait_lgkm0() { asm volatile("s_waitcnt lgkmcnt(0)" ::: "memory"); }
template <int N> DI void wait_lgkm() { asm volatile("s_waitcnt lgkmcnt(%0)" ::"n"(N > 15 ? 15 : N) : "memory"); }
DI void bar() { asm volatile("s_barrier" ::: "memory"); }
DI unsigned cvt_pk_bf16(float lo, float hi) {
  typedef __bf16 bf16x2 __attribute__((ext_vector_type(2)));
  f32x2 v = {lo, hi}; bf16x2 b = __builtin_convertvector(v, bf16x2); return __builtin_bit_cast(unsigned, b);
}
DI float bf_lo(unsigned w) { return __uint_as_float(w << 16); }
DI float bf_hi(unsigned w) { return __uint_as_float(w & 0xffff0000u); }
DI float rbf(float v) { return bf_lo(cvt_pk_bf16(v, v)); }
template <int I, int N, class F> DI void static_for(F&& f) { if constexpr (I < N) { f(std::integral_constant<int, I>{}); static_for<I + 1, N>(f); } }

#define ACLOB "a0","a1","a2","a3","a4","a5","a6","a7","a8","a9","a10","a11","a12","a13","a14","a15","a16","a17","a18","a19","a20","a21","a22","a23","a24","a25","a26","a27","a28","a29","a30","a31","a32","a33","a34","a35","a36","a37","a38","a39","a40","a41","a42","a43","a44","a45","a46","a47","a48","a49","a50","a51","a52","a53","a54","a55","a56","a57","a58","a59","a60","a61","a62","a63","a64","a65","a66","a67","a68","a69","a70","a71","a72","a73","a74","a75","a76","a77","a78","a79","a80","a81","a82","a83","a84","a85","a86","a87","a88","a89","a90","a91","a92","a93","a94","a95","a96","a97","a98","a99","a100","a101","a102","a103","a104","a105","a106","a107","a108","a109","a110","a111","a112","a113","a114","a115","a116","a117","a118","a119","a120","a121","a122","a123","a124","a125","a126","a127","a128","a129","a130","a131","a132","a133","a134","a135","a136","a137","a138","a139","a140","a141","a142","a143","a144","a145","a146","a147","a148","a149","a150","a151","a152","a153","a154","a155","a156","a157","a158","a159","a160","a161","a162","a163","a164","a165","a166","a167","a168","a169","a170","a171","a172","a173","a174","a175","a176","a177","a178","a179","a180","a181","a182","a183","a184","a185","a186","a187","a188","a189","a190","a191"
template <int RA, int RB> DI void mfma16(f32x4& acc) { asm volatile("v_mfma_f32_16x16x32_bf16 %0, a[%c1:%c2], a[%c3:%c4], %0" : "+v"(acc) : "n"(RA), "n"(RA + 3), "n"(RB), "n"(RB + 3)); }
template <int RD, int OFF> DI void ds_r128(unsigned addr) { asm volatile("ds_read_b128 a[%c1:%c2], %0 offset:%c3" ::"v"(addr), "n"(RD), "n"(RD + 3), "n"(OFF) : "memory"); }
template <int RD, int OFF> DI void ds_rtr(unsigned addr) { asm volatile("ds_read_b64_tr_b16 a[%c1:%c2], %0 offset:%c3" ::"v"(addr), "n"(RD), "n"(RD + 1), "n"(OFF) : "memory"); }
DI void wait_frags() { asm volatile("s_waitcnt lgkmcnt(0)" ::: "memory", ACLOB); }
template <int OFF, int POL> DI void dma16(int voff, __amdgpu_buffer_rsrc_t r, unsigned soff) {
  if constexpr (POL == 0) asm volatile("buffer_load_dwordx4 %0, %1, %2 offen offset:%3 lds" ::"v"(voff), "s"(r), "s"(soff), "n"(OFF) : "memory");
  if constexpr (POL == 1) asm volatile("buffer_load_dwordx4 %0, %1, %2 offen offset:%3 sc1 lds" ::"v"(voff), "s"(r), "s"(soff), "n"(OFF) : "memory");
  if constexpr (POL == 2) asm volatile("buffer_load_dwordx4 %0, %1, %2 offen offset:%3 nt lds" ::"v"(voff), "s"(r), "s"(soff), "n"(OFF) : "memory");
}
DI void pinacc(f32x4& v) { asm volatile("" : "+v"(v)); }
template <int C> DI int s_eq(int v) { int r; asm volatile("s_cmp_eq_u32 %1, %2\n\ts_cselect_b32 %0, 1, 0" : "=s"(r) : "s"(v), "n"(C) : "scc"); return r; }
DI __amdgpu_buffer_rsrc_t mkrsrc(const void* p, unsigned bytes) { return __builtin_amdgcn_make_buffer_rsrc((void*)p, 0, bytes, 0x00020000); }

#define STAMP(k, i) do {} while (0)
#define STAGGER 0
#define SNAKE 1
#define LN_PASS0 0
enum { ROWK = 0, FRAG = 1, NMAJ = 2 };

template <int KIND, int R, int POL> struct Stream {
  static constexpr bool ODD = KIND == NMAJ && R == 224;
  static constexpr int P = R / 32, NG = (P + 3) / 4, NV = ODD ? 7 : (KIND == FRAG ? 2 : 4);
  __amdgpu_buffer_rsrc_t rs;
  int voff[NV];
  unsigned s0[NG], ks;
  unsigned ml[NG];
  static DI int gpiece(int g, int wid) { if constexpr (ODD) return wid * 7 + 4 * g; else return 16 * g + 4 * wid; }
  DI void init_rowk(const void* p, unsigned bytes, int ld, int r0, int k0, int wid, int lane) {
    rs = mkrsrc(p, bytes);
#pragma unroll
    for (int j = 0; j < 4; ++j) voff[j] = ((lane >> 3) + 8 * j) * ld * 2 + (((lane & 7) ^ ((4 * (j & 1) + (lane >> 4)) & 7)) * 16) - j * 1024;
#pragma unroll
    for (int g = 0; g < NG; ++g) s0[g] = (unsigned)((r0 + 8 * gpiece(g, wid)) * ld + k0) * 2u;
    ks = 128u;
  }
  DI void init_frag(const void* p, unsigned bytes, int kbn, int r0, int k0, int wid, int lane) {
    rs = mkrsrc(p, bytes);
    voff[0] = lane * 16; voff[1] = lane * 16 + kbn * 1024 - 2048;
#pragma unroll
    for (int g = 0; g < NG; ++g) s0[g] = (unsigned)((r0 / 16 + gpiece(g, wid) / 2) * kbn + k0 / 32) * 1024u;
    ks = 2048u;
  }
  DI void init_nmaj(const void* p, unsigned bytes, int ld, int n0, int k0, int wid, int lane) {
    rs = mkrsrc(p, bytes);
    if constexpr (ODD) {
#pragma unroll
      for (int j = 0; j < 7; ++j) { const int L = (wid * 7 + j) * 64 + lane, kk = L / 28, pos = L % 28;
        voff[j] = kk * ld * 2 + (pos ^ (((kk >> 3) & 1) << 1)) * 16 - (j & 3) * 1024; }
      s0[0] = s0[1] = (unsigned)(k0 * ld + n0) * 2u;
    } else {
      static_assert(R == 128 || ODD, "");
#pragma unroll
      for (int j = 0; j < 4; ++j) { const int kk = 4 * (4 * wid + j) + (lane >> 4), g = (kk & 3) | (((kk >> 3) & 1) << 2);
        voff[j] = (4 * j + (lane >> 4)) * ld * 2 + (((lane & 15) ^ (g << 1)) * 16) - j * 1024; }
      s0[0] = (unsigned)((k0 + 16 * wid) * ld + n0) * 2u;
    }
    ks = (unsigned)(64 * ld) * 2u;
  }
  DI void init_lds(unsigned base, int wid) {
#pragma unroll
    for (int g = 0; g < NG; ++g) ml[g] = base + (unsigned)gpiece(g, wid) * 1024u;
  }
  template <int I, int SO> DI void m0() const { asm volatile("s_add_u32 m0, %0, %1" ::"s"(ml[I / 4]), "n"(SO) : "memory", "scc"); }
  template <int I> DI void issue() const {
    constexpr int g = I / 4, j = I % 4, vi = ODD ? I : (KIND == FRAG ? (j >> 1) : j);
    dma16<j * 1024, POL>(voff[vi], rs, s0[g]);
  }
  unsigned se[NG];
  DI void init_end(int nt) {
#pragma unroll
    for (int g = 0; g < NG; ++g) se[g] = s0[g] + nt * ks;
  }
  template <int I> DI void advance() { asm volatile("s_add_u32 %0, %0, %1\n\ts_cmp_eq_u32 %0, %2\n\ts_cselect_b32 %0, 2.0, %0" : "+s"(s0[I / 4]) : "s"(ks), "s"(se[I / 4]) : "scc"); }
};

template <int KIND, int R, int T, int NS, int STAGE> struct Reader {
  static constexpr int NA = KIND == FRAG ? 1 : (KIND == ROWK ? 2 : 3);
  static constexpr int SPAN = KIND == NMAJ ? 64 * R * 2 : T * 2048, SPW = (65536 - SPAN) / STAGE + 1, NW = (NS + SPW - 1) / SPW;
  unsigned a[NW][NA];
  DI void init(unsigned base, int t0, int lane) {
    const int fr = lane & 15, fq = lane >> 4;
    if constexpr (KIND == ROWK) { a[0][0] = base + (t0 * 16 + fr) * 128 + ((fq ^ (fr >> 1)) * 16); a[0][1] = base + (t0 * 16 + fr) * 128 + (((4 + fq) ^ (fr >> 1)) * 16); }
    else if constexpr (KIND == FRAG) a[0][0] = base + t0 * 2048 + lane * 16;
    else { static_assert(KIND != NMAJ || R == 224, "");
      const int q = fr >> 2, p = lane & 3, wn = t0 / 7; const unsigned b = base + (8 * fq + q) * 448 + 8 * p;
      a[0][0] = b + 32 * ((6 * wn) ^ (fq & 1)); a[0][1] = b + 32 * ((6 * wn + 1) ^ (fq & 1)); a[0][2] = b + 32 * ((12 + wn) ^ (fq & 1)); }
#pragma unroll
    for (int w = 1; w < NW; ++w)
#pragma unroll
      for (int n = 0; n < NA; ++n) { a[w][n] = a[0][n] + w * SPW * STAGE; asm volatile("" : "+v"(a[w][n])); }
  }
  template <int ST, int KS, int I, int RB> DI void rd() const {
    constexpr int w = ST / SPW, so = (ST % SPW) * STAGE;
    if constexpr (KIND == ROWK) ds_r128<RB + 4 * I, so + I * 2048>(a[w][KS]);
    else if constexpr (KIND == FRAG) ds_r128<RB + 4 * I, so + I * 2048 + KS * 1024>(a[w][0]);
    else { constexpr int n = I / 2, h = I % 2; ds_rtr<RB + 4 * n + 2 * h, so + (32 * KS + 4 * h) * (R * 2) + (n < 6 ? 32 * (n & ~1) : 0)>(a[w][n < 6 ? (n & 1) : 2]); }
  }
};

template <int NTILE_, int BM_, int BN_, int NS_, int KA_, int KB_, int PLA_, int PLB_, bool TR_, int NX_, bool LAZY_, int RSP_> struct Geo {
  static constexpr bool LAZY = LAZY_; static constexpr int NTILE = NTILE_, NX = NX_, PLA = PLA_, PLB = PLB_;
  static constexpr int BM = BM_, BN = BN_, NS = NS_, KA = KA_, KB = KB_, MT = BM / 32, NT = BN / 32, NM = MT * NT;
  static constexpr bool TR = TR_;
  static constexpr int PA = BM / 32, PB = BN / 32, ND = PA + PB, STAGE = (BM + BN) * 128;
  static constexpr int DEPTH = LAZY ? NS - 1 : NS;
  static constexpr int H = (DEPTH == 2) ? ND : ND / 2;
  static constexpr int NIA = KA == NMAJ ? 2 * MT : MT, NIB = KB == NMAJ ? 2 * NT : NT, NR = NIA + NIB, RSPAN = RSP_;
  static constexpr int rgap(int r) { return (r * RSPAN) / NR; }
  static constexpr int rbefore(int i) { int c = 0; for (int r = 0; r < NR; ++r) c += rgap(r) < i; return c; }
  typedef Reader<KA, BM, MT, NS, STAGE> RA; typedef Reader<KB, BN, NT, NS, STAGE> RB;
};
struct Side { __amdgpu_buffer_rsrc_t rs; unsigned voff, step, soff; };
template <class G> struct Pipe {
  Stream<G::KA, G::BM, G::PLA> A; Stream<G::KB, G::BN, G::PLB> B;
  Side xl, x2;
  unsigned ls = 0;
  unsigned* sig = nullptr; unsigned tag = 0;
  unsigned lds; int wid;
  DI void side() {
    if constexpr (G::NX > 0) asm volatile("buffer_load_dword a124, %0, %1, %2 offen" ::"v"(xl.voff), "s"(xl.rs), "s"(xl.soff) : "memory");
    if constexpr (G::NX > 1) asm volatile("buffer_load_dword a124, %0, %1, %2 offen" ::"v"(x2.voff), "s"(x2.rs), "s"(x2.soff) : "memory"); }
  DI void side_adv() {
    if constexpr (G::NX > 0) asm volatile("s_add_u32 %0, %0, %1" : "+s"(xl.soff) : "s"(xl.step) : "scc");
    if constexpr (G::NX > 1) asm volatile("s_add_u32 %0, %0, %1" : "+s"(x2.soff) : "s"(x2.step) : "scc"); }
  static constexpr bool gstart(int i) { return i < G::PA ? (i % 4 == 0) : ((i - G::PA) % 4 == 0); }
  static constexpr bool gend(int i) { return i == G::PA - 1 || i == G::ND - 1 || gstart(i + 1); }
  template <int I, int ST> DI void m0() const { if constexpr (I < G::PA) A.template m0<I, ST * G::STAGE>(); else B.template m0<I - G::PA, ST * G::STAGE>(); }
  template <int I> DI void issue() const { if constexpr (I < G::PA) A.template issue<I>(); else B.template issue<I - G::PA>(); }
  template <int I> DI void advance() { if constexpr (I < G::PA) A.template advance<I>(); else B.template advance<I - G::PA>(); }
  template <int MASK, bool LAST> DI void prologue() {
    if constexpr (MASK & 1) { A.init_lds(lds, wid); A.init_end(G::NTILE); }
    if constexpr (MASK & 2) { B.init_lds(lds + G::BM * 128, wid); B.init_end(G::NTILE); }
    asm volatile("s_nop 4" ::: "memory", ACLOB);
    static_for<0, G::DEPTH>([&](auto Ss) {
      if constexpr (LAST && Ss.value >= 2) { side(); side_adv(); }
      static_for<0, (Ss.value == G::DEPTH - 1 ? G::H : G::ND)>([&](auto Dd) { constexpr int d = Dd.value;
        if constexpr ((MASK >> (d < G::PA ? 0 : 1)) & 1) {
          if constexpr (gstart(d)) { m0<d, Ss.value>(); asm volatile("s_nop 0"); }
          issue<d>();
          if constexpr (gend(d)) advance<d>(); }
        else if constexpr (LAST && Ss.value >= 1) asm volatile("buffer_load_dword a124, off, %0, %1" ::"s"(A.rs), "s"(OOB) : "memory"); }); });
    if constexpr (LAST && G::H < G::ND) if constexpr (!gstart(G::H)) m0<G::H, G::DEPTH - 1>();
  }
};

constexpr int dgap(int d, int ndp, int nm, int w) { return 1 + (d * (nm - 2) + w * (nm - 2) / 4) / ndp; }
constexpr int dat(int i, int ndp, int nm, int w) { for (int d = 0; d < ndp; ++d) if (dgap(d, ndp, nm, w) == i) return d; return -1; }
template <class G, int W, int CUR, int RST, int RKS, int D0, int D1, int DST, int I>
DI void gap(Pipe<G>& p, const typename G::RA& ra, const typename G::RB& rb, f32x4 (&acc)[G::MT][G::NT]) {
  constexpr int CA = 64 * CUR, CB = CA + 32, NA = 64 * (CUR ^ 1), NB = NA + 32;
  constexpr int NM = G::NM, NIB = G::NIB, NR = G::NR, NDP = D1 - D0, m = I / G::NT, n0 = I % G::NT, n = (SNAKE && (m & 1)) ? G::NT - 1 - n0 : n0;
  constexpr int d = dat(I, NDP, NM, W), dp = dat(I - 1, NDP, NM, W);
  if constexpr (n0 == 0 && m % 2 == 0 && (G::LAZY || CUR == 0)) wait_lgkm<(NR - 1 - (NIB + m + 1)) + G::rbefore(I)>();
  if constexpr (d >= 0) if constexpr (Pipe<G>::gstart(D0 + d)) p.template m0<D0 + d, DST>();
  if constexpr (G::TR) mfma16<CB + 4 * n, CA + 4 * m>(acc[m][n]); else mfma16<CA + 4 * m, CB + 4 * n>(acc[m][n]);
  if constexpr (I == 0 && CUR == (G::DEPTH == 2 ? 0 : 1)) p.side();
  if constexpr (I == NM - 1 && CUR == (G::DEPTH == 2 ? 0 : 1)) p.side_adv();
  if constexpr (dp >= 0) if constexpr (Pipe<G>::gend(D0 + dp)) p.template advance<D0 + dp>();
  if constexpr (d >= 0) p.template issue<D0 + d>();
  static_for<G::rbefore(I), G::rbefore(I + 1)>([&](auto Rr) { constexpr int r = Rr.value;
    if constexpr (r == 0) ra.template rd<RST, RKS, 0, NA>();
    else if constexpr (r <= NIB) rb.template rd<RST, RKS, r - 1, NB>();
    else ra.template rd<RST, RKS, r - NIB, NA>(); });
}
template <class G, int W, int CUR, int RST, int RKS, int D0, int D1, int DST, int... Is>
DI void phase_(Pipe<G>& p, const typename G::RA& ra, const typename G::RB& rb, f32x4 (&acc)[G::MT][G::NT], std::integer_sequence<int, Is...>) {
  (gap<G, W, CUR, RST, RKS, D0, D1, DST, Is>(p, ra, rb, acc), ...);
}
template <class G, int W, int ST>
DI void ktile(Pipe<G>& p, const typename G::RA& ra, const typename G::RB& rb, f32x4 (&acc)[G::MT][G::NT]) {
  constexpr int NS = G::NS, ND = G::ND, H = G::H, DP = G::DEPTH;
  phase_<G, W, 0, ST, 1, H, ND, (ST + DP - 1) % NS>(p, ra, rb, acc, std::make_integer_sequence<int, G::NM>{});
  if constexpr (G::LAZY) asm volatile("s_waitcnt vmcnt(%0)\n\ts_barrier" ::"n"((DP - 2) * (ND + G::NX)) : "memory", ACLOB);
  else asm volatile("s_waitcnt vmcnt(%0) lgkmcnt(0)\n\ts_barrier" ::"n"(DP == 2 ? G::NX : (DP - 2) * (ND + G::NX)) : "memory", ACLOB);
  phase_<G, W, 1, (ST + 1) % NS, 0, 0, H, (ST + DP) % NS>(p, ra, rb, acc, std::make_integer_sequence<int, G::NM>{});
}
template <class G, int W, int NTILE, class Hook>
DI void gemm_body(Pipe<G>& p, const typename G::RA& ra, const typename G::RB& rb, f32x4 (&acc)[G::MT][G::NT], int on, Hook hook) {
#pragma nounroll
  for (int it = on * (NTILE / G::NS); it > 0; --it) { static_for<0, G::NS>([&](auto Ss) { ktile<G, W, Ss.value>(p, ra, rb, acc); });
    hook(it);
  }
  if constexpr (NTILE % G::NS != 0) {
    asm volatile("" : "+s"(on));
#pragma nounroll
    for (int it = on; it > 0; --it) static_for<0, NTILE % G::NS>([&](auto Ss) { ktile<G, W, Ss.value>(p, ra, rb, acc); });
  }
}
template <class G, int NTILE, class Init, class Hook>
DI void gemm_loop(Pipe<G>& p, int lane, f32x4 (&acc)[G::MT][G::NT], int st0, Init init, Hook hook) {
  constexpr int ND = G::ND, H = G::H, DP = G::DEPTH;
  typename G::RA ra; typename G::RB rb;
  ra.init(p.lds, (p.wid >> 1) * G::MT, lane); rb.init(p.lds + G::BM * 128, (p.wid & 1) * G::NT, lane);
#pragma unroll
  for (int n = 0; n < G::NT; ++n) { const f32x4 v = init(n);
#pragma unroll
    for (int m = 0; m < G::MT; ++m) { acc[m][n] = v; pinacc(acc[m][n]); } }
  wait_vm<(DP - 2) * (ND + G::NX) + H>();
  bar();
  if (p.sig != nullptr && threadIdx.x == 0) __hip_atomic_store(p.sig, p.tag, __ATOMIC_RELAXED, __HIP_MEMORY_SCOPE_AGENT);
  static_for<0, G::NIA>([&](auto I) { ra.template rd<0, 0, I.value, 0>(); });
  static_for<0, G::NIB>([&](auto I) { rb.template rd<0, 0, I.value, 32>(); });
  wait_frags();
  STAMP(1, st0);
  asm volatile("s_nop 1");
  gemm_body<G, 0, NTILE>(p, ra, rb, acc, 1, hook);
  asm volatile("s_nop 7\n\ts_nop 7\n\ts_waitcnt vmcnt(0) lgkmcnt(0)" ::: "memory", ACLOB);
#pragma unroll
  for (int m = 0; m < G::MT; ++m)
#pragma unroll
    for (int n = 0; n < G::NT; ++n) pinacc(acc[m][n]);
  STAMP(1, st0 + 1);
}

DI void touch(const void* p) { asm volatile("global_load_dword a124, %0, off" ::"v"(p) : "memory"); }
template <int R, int OFF> DI void gld4_nt(const void* p) { asm volatile("global_load_dwordx4 a[%c1:%c2], %0, off offset:%c3 nt" ::"v"(p), "n"(R), "n"(R + 3), "n"(OFF) : "memory"); }
template <int R> DI void gld2(const void* p) { asm volatile("global_load_dwordx2 a[%c1:%c2], %0, off" ::"v"(p), "n"(R), "n"(R + 1) : "memory"); }
template <int R, int OFF> DI void bld4_dev(__amdgpu_buffer_rsrc_t r, unsigned voff, unsigned soff) { asm volatile("buffer_load_dwordx4 a[%c3:%c4], %0, %1, %2 offen offset:%c5 sc1" ::"v"(voff), "s"(r), "s"(soff), "n"(R), "n"(R + 3), "n"(OFF) : "memory"); }
template <int R> DI void flag_req(const unsigned* p) { asm volatile("global_load_dword a[%c1], %0, off sc1" ::"v"(p), "n"(R) : "memory"); }
template <int R> DI unsigned ard() { unsigned r; asm volatile("v_accvgpr_read_b32 %0, a[%c1]" : "=v"(r) : "n"(R)); return r; }
template <int R> DI u32x4 ard4() { return (u32x4){ard<R>(), ard<R + 1>(), ard<R + 2>(), ard<R + 3>()}; }
template <int R> DI u32x2 ard2() { return (u32x2){ard<R>(), ard<R + 1>()}; }
template <int CTRL> DI float dpp_add(float a) { return a + __builtin_bit_cast(float, __builtin_amdgcn_update_dpp(0, __builtin_bit_cast(int, a), CTRL, 0xf, 0xf, true)); }
template <int N> DI void wave_sum(float (&s)[N]) {
#pragma unroll
  for (int r = 0; r < N; ++r) s[r] = dpp_add<0xB1>(s[r]);
#pragma unroll
  for (int r = 0; r < N; ++r) s[r] = dpp_add<0x4E>(s[r]);
#pragma unroll
  for (int r = 0; r < N; ++r) s[r] = dpp_add<0x141>(s[r]);
#pragma unroll
  for (int r = 0; r < N; ++r) s[r] = dpp_add<0x140>(s[r]);
#pragma unroll
  for (int r = 0; r < N; ++r) { const auto p = __builtin_amdgcn_permlane16_swap(__float_as_uint(s[r]), __float_as_uint(s[r]), false, false); s[r] = __uint_as_float(p[0]) + __uint_as_float(p[1]); }
#pragma unroll
  for (int r = 0; r < N; ++r) { const auto p = __builtin_amdgcn_permlane32_swap(__float_as_uint(s[r]), __float_as_uint(s[r]), false, false); s[r] = __uint_as_float(p[0]) + __uint_as_float(p[1]); }
}

DI f32x2 sigm_mul(f32x2 x, f32x2 arg) {
  f32x2 e; e.x = __builtin_amdgcn_exp2f(arg.x); e.y = __builtin_amdgcn_exp2f(arg.y);
  const f32x2 d = e + 1.0f;
  f32x2 r; r.x = __builtin_amdgcn_rcpf(d.x); r.y = __builtin_amdgcn_rcpf(d.y);
  return x * r;
}
DI f32x2 gelu2(f32x2 x) {
  const float k0 = -2.0f * 0.7978845608028654f * 1.4426950408889634f, k1 = k0 * 0.044715f;
  return sigm_mul(x, x * ((x * x) * k1 + k0));
}
DI f32x2 silu2(f32x2 x) { return sigm_mul(x, x * -1.4426950408889634f); }
DI f32x4 gelu4(f32x4 t) { const f32x2 a = gelu2((f32x2){t[0], t[1]}), b = gelu2((f32x2){t[2], t[3]}); return (f32x4){a.x, a.y, b.x, b.y}; }
DI f32x4 bf4(u32x2 w) { return (f32x4){bf_lo(w[0]), bf_hi(w[0]), bf_lo(w[1]), bf_hi(w[1])}; }

#define GO_NS 4
#define GO_LAZY false
#define GO_RSP 11
#define DEVPOL 1
typedef Geo<16, 256, 128, 3, FRAG, ROWK, DEVPOL, 0, false, 1, false, 24> GAB;
#define YT1 3
#define YT2 11
#define ABT 4
#define PREP_DELAY 16
#define PREP_DELAY2 0
#define PREP_FIRST 0
#define EP1 1
#define EP2 5
#define ABS 8
#define YS0 5
#define YSN 5
#define WS2 1
#define HS0 3
#define HSN 8
typedef Geo<32, 256, 128, 3, ROWK, FRAG, 0, DEVPOL, true, WS2 == 0, false, 24> GY;
#define H_TOUCH (ABT < 4)
typedef Geo<16, 256, 224, 2, FRAG, NMAJ, DEVPOL, 0, true, WS2 == 1, false, 42> GH;
typedef Geo<56, 128, 128, GO_NS, FRAG, FRAG, DEVPOL, DEVPOL, true, 0, GO_LAZY, GO_RSP> GO;
constexpr int LDSM = GY::NS * GY::STAGE > GO::NS * GO::STAGE ? GY::NS * GY::STAGE : GO::NS * GO::STAGE;

DI u32x4 pack_pair(const f32x4& a, const f32x4& b) {
  const unsigned a0 = cvt_pk_bf16(a[0], a[1]), a1 = cvt_pk_bf16(a[2], a[3]), b0 = cvt_pk_bf16(b[0], b[1]), b1 = cvt_pk_bf16(b[2], b[3]);
  const auto r0 = __builtin_amdgcn_permlane16_swap(a0, b0, false, false);
  const auto r1 = __builtin_amdgcn_permlane16_swap(a1, b1, false, false);
  return (u32x4){r0[0], r1[0], r0[1], r1[1]};
}
DI u32x4 pack8(const f32x4& a, const f32x4& b) { return (u32x4){cvt_pk_bf16(a[0], a[1]), cvt_pk_bf16(a[2], a[3]), cvt_pk_bf16(b[0], b[1]), cvt_pk_bf16(b[2], b[3])}; }
DI unsigned hpos(int t, int b) { return b == 0 ? 2 * t : (b == 3 ? 2 * t + 1 : 32 + 5 * t + (b < 3 ? b - 1 : b - 2)); }
struct NoHook { DI void operator()(int) const {} };
struct ZeroInit { DI f32x4 operator()(int) const { return (f32x4){0.f, 0.f, 0.f, 0.f}; } };

DI void st_dev(__amdgpu_buffer_rsrc_t r, unsigned off, u32x4 v) { __builtin_amdgcn_raw_buffer_store_b128(v, r, off, 0, 16); }
DI void st_dev8(__amdgpu_buffer_rsrc_t r, unsigned off, u32x2 v) { __builtin_amdgcn_raw_buffer_store_b64(v, r, off, 0, 16); }
DI u32x4 ld_dev(__amdgpu_buffer_rsrc_t r, unsigned voff, unsigned soff) { return __builtin_amdgcn_raw_buffer_load_b128(r, voff, soff, 16); }
DI void signal(unsigned* f, unsigned tag) { if (threadIdx.x == 0) __hip_atomic_store(f, tag, __ATOMIC_RELAXED, __HIP_MEMORY_SCOPE_AGENT); }
DI void publish(unsigned* f, unsigned tag) { wait_vm<0>(); bar(); signal(f, tag); }
DI void poll(unsigned* f, int n, unsigned tag) {
  unsigned* q = f + ((int)(threadIdx.x & 63) < n ? (threadIdx.x & 63) : 0) * FS;
#pragma nounroll
  for (int i = 0; i < SPIN_CAP && !__all(__hip_atomic_load(q, __ATOMIC_RELAXED, __HIP_MEMORY_SCOPE_AGENT) == tag); ++i) __builtin_amdgcn_s_sleep(1);
}
DI void await(unsigned* f, int n, unsigned tag) {
  if (threadIdx.x < 64) poll(f, n, tag);
  asm volatile("" ::: "memory");
  bar();
}

__global__ __launch_bounds__(256, 1) void k_main(const u16* __restrict__ x, const u16* __restrict__ ws_re, const u16* __restrict__ ws_im, const u16* __restrict__ wd_re, const u16* __restrict__ wd_im,
                                                 const u16* __restrict__ w1, const u16* __restrict__ b1, const u16* __restrict__ w2, const u16* __restrict__ b2, unsigned char* ws, u16* __restrict__ out) {
  extern __shared__ __attribute__((aligned(16))) unsigned char shm[];
  {
    const char* pc; unsigned len;
    asm volatile("s_getpc_b64 %0\n.Lkm_pc:\n\ts_mov_b32 %1, .Lkm_endy-.Lkm_pc\n\ts_max_i32 %1, %1, .Lkm_endo-.Lkm_pc" : "=s"(pc), "=s"(len) :: "scc");
    asm volatile("buffer_load_dword a124, %0, %1, 0 offen" ::"v"((((blockIdx.x >> 3) & 1) * 256 + threadIdx.x) * 128), "s"(mkrsrc(pc, len)) : "memory", ACLOB);
  }
  const int wid = __builtin_amdgcn_readfirstlane(threadIdx.x >> 6), lane = threadIdx.x & 63, fr = lane & 15, fq = lane >> 4, wm = wid >> 1, wn = wid & 1;
  const int xcd = blockIdx.x & 7, slot = blockIdx.x >> 3, chain = slot >> 4, s4 = slot & 15;
  const int pm1 = (xcd & 1) * 4 + (s4 & 3), pn1 = (xcd >> 1) * 4 + (s4 >> 2);
  const int half = xcd & 1, pm = (xcd >> 2) * 4 + (s4 & 3), pn = ((xcd >> 1) & 1) * 4 + (s4 >> 2), tile = pm * 8 + pn;
  const unsigned ldsb = (unsigned)(size_t)(LAS unsigned char*)shm;
  const int lo = ((fq & 1) * 2 + (fq >> 1)) * 256 + fr * 16;
  unsigned* const flag0 = (unsigned*)(ws + WS_FLAG);
#define FLAG(i) (flag0 + (i) * FS)
  const __amdgpu_buffer_rsrc_t ry = mkrsrc(ws + WS_PY + (size_t)tile * 131072, 131072);
  STAMP(1, 0);
  const int kb = xcd * 16 + s4;
  unsigned tag;
  {
    const u16* px = x + (size_t)(blockIdx.x * 8 + wid * 2) * D_ + lane * 8;
    asm volatile("global_load_dword a16, %0, off sc1" ::"v"(FLAG(FL_E)) : "memory");
    gld4_nt<0, 0>(px); gld4_nt<4, 1024>(px); gld4_nt<8, 2048>(px); gld4_nt<12, 3072>(px); }
  auto layernorm = [&](auto NY) {
    wait_vm<NY.value>();
    const u32x4 raw[4] = {ard4<0>(), ard4<4>(), ard4<8>(), ard4<12>()};
    const unsigned ev = ard<16>();
    f32x2 v[2][8]; float sm[2], mean[2];
#pragma unroll
    for (int i = 0; i < 16; ++i) v[i >> 3][i & 7] = (f32x2){bf_lo(raw[i >> 2][i & 3]), bf_hi(raw[i >> 2][i & 3])};
#pragma unroll
    for (int r = 0; r < 2; ++r) { const f32x2 a = ((v[r][0] + v[r][1]) + (v[r][2] + v[r][3])) + ((v[r][4] + v[r][5]) + (v[r][6] + v[r][7])); sm[r] = a.x + a.y; }
    wave_sum(sm);
#pragma unroll
    for (int r = 0; r < 2; ++r) { mean[r] = sm[r] * (1.0f / D_); f32x2 a = {0.f, 0.f};
#pragma unroll
      for (int e = 0; e < 8; ++e) { const f32x2 dd = v[r][e] - mean[r]; a += dd * dd; }
      sm[r] = a.x + a.y; }
    wave_sum(sm);
    LAS unsigned char* tl = (LAS unsigned char*)shm;
#pragma unroll
    for (int r = 0; r < 2; ++r) { const float rden = 1.0f / rbf(sqrtf(rbf(rbf(sm[r] * (1.0f / D_)) + 1e-5f))), mb = rbf(mean[r]);
#pragma unroll
      for (int j = 0; j < 2; ++j) { u32x4 o;
#pragma unroll
        for (int e = 0; e < 4; ++e) { const f32x2 d = v[r][4 * j + e] - mb; const unsigned db = cvt_pk_bf16(d.x, d.y); const f32x2 q = (f32x2){bf_lo(db), bf_hi(db)} * rden; o[e] = cvt_pk_bf16(q.x, q.y); }
        *(LAS u32x4*)(tl + (j * 64 + lane) * 128 + ((wid * 2 + r + lane) & 7) * 16) = o; } }
    tag = __builtin_amdgcn_readfirstlane(ev) + 1u;
    wait_lgkm0(); bar();
    STAMP(1, 14);
    const __amdgpu_buffer_rsrc_t rx = mkrsrc(ws + WS_XA + (size_t)(blockIdx.x >> 1) * 32768, 32768);
#pragma unroll
    for (int j = 0; j < 4; ++j) { const int ch = (wid * 4 + j) * 8 + (lane >> 3);
      st_dev(rx, ch * 256 + (blockIdx.x & 1) * 128 + (lane & 7) * 16, *(const LAS u32x4*)(tl + ch * 128 + ((lane + ch) & 7) * 16)); }
    publish(FLAG(FL_X + blockIdx.x), tag);
    STAMP(1, 10);
  };
  if (chain == 0) {
    const u16* wsm = half ? ws_im : ws_re;
    const u16* wshare = wsm + (size_t)(pm * 256 + pn * 32) * S_;
    f32x4 acc[8][4];
    {
      Pipe<GAB> p; p.lds = ldsb; p.wid = wid;
      p.B.init_rowk(pn1 < 8 ? wd_re : wd_im, D_ * D_ * 2, D_, (pn1 & 7) * 128, 0, wid, lane);
      p.prologue<2, false>();
      layernorm(std::integral_constant<int, 8>{});
      __builtin_amdgcn_s_sleep(PREP_DELAY);
      {
        const int t = (s4 * 8 + xcd) * 256 + threadIdx.x;
        const int c = (s4 & 3) * 256 + threadIdx.x;
        if (t < PREP_FIRST) touch((const char*)w1 + (size_t)(t + 7168) * 128);
        __builtin_amdgcn_s_sleep(PREP_DELAY2);
        touch((const char*)w1 + (size_t)t * 128);
        touch(t < 24576 ? (const char*)w1 + (size_t)(t + 32768) * 128 : (c < 512 ? (const char*)b1 + (c % 56) * 128 : (const char*)b2 + (c & 15) * 128));
        touch((const char*)(t < 16384 ? wd_re : wd_im) + (size_t)(t & 16383) * 128);
        const __amdgpu_buffer_rsrc_t rs = mkrsrc(w2 + (size_t)(kb < 112 ? kb : 0) * 32 * D_, kb < 112 ? 32 * D_ * 2 : 0);
        asm volatile("s_nop 4" ::: "memory");
        static_for<0, 16>([&](auto I) { constexpr int i = I.value;
          const int g = ((i >> 1) & 3) | ((wid & 1) << 2);
          asm volatile("s_add_u32 m0, %0, %1\n\ts_nop 0" ::"s"(ldsb + (1 + wm) * GAB::STAGE + wn * 16384), "n"(i * 1024) : "memory", "scc");
          dma16<0, 2>((lane ^ (g << 1)) * 16, rs, wid * 16384 + i * 1024); });
      }
      wait_vm<0>(); bar();
      if (kb < 112) {
        const int q = fr >> 2, gk = q | ((fq & 1) << 2);
        const __amdgpu_buffer_rsrc_t rw = mkrsrc(ws + WS_W2, F_ * D_ * 2);
        LAS unsigned char* src = (LAS unsigned char*)shm + (1 + (fq >> 1)) * GAB::STAGE + (8 * (fq & 1) + q) * 2048 + wid * 512 + 8 * (lane & 3);
        const unsigned kp = hpos(kb / 7, kb % 7);
#pragma unroll
        for (int n = 0; n < 16; ++n) {
          const bf16x4 l4 = __builtin_amdgcn_ds_read_tr16_b64_v4i16((LAS bf16x4*)(src + 32 * (n ^ gk)));
          const bf16x4 h4 = __builtin_amdgcn_ds_read_tr16_b64_v4i16((LAS bf16x4*)(src + 32 * (n ^ gk) + 8192));
          const u32x2 a = __builtin_bit_cast(u32x2, l4), b = __builtin_bit_cast(u32x2, h4);
          st_dev(rw, (unsigned)((wid * 16 + n) * 112 + kp) * 1024u + lane * 16, (u32x4){a[0], a[1], b[0], b[1]});
        }
        wait_lgkm0();
      }
      STAMP(1, 4);
      p.xl.rs = mkrsrc(wshare + (size_t)wid * 8 * S_, 32768); p.xl.step = 16384; p.xl.soff = 0u - (unsigned)(kb % ABS) * 16384u; p.xl.voff = fq * 4096 + fr * 128;
      p.A.init_frag(ws + WS_XA, S_ * D_ * 2, 32, pm1 * 256, 0, wid, lane);
      await(FLAG(FL_X + pm1 * 32), 32, tag);
      p.prologue<1, true>();
      p.sig = FLAG(FL_W2 + kb); p.tag = tag;
      gemm_loop<GAB, 16>(p, lane, acc, 1, ZeroInit(), NoHook());
      const __amdgpu_buffer_rsrc_t ro = mkrsrc(ws + WS_AB, S_ * 2 * D_ * 2);
      const unsigned ob = (unsigned)((pn1 * 8 + wn * 4) * 64 + pm1 * 8 + wm * 4) * 1024u + lo;
#pragma unroll
      for (int n = 0; n < 4; ++n)
#pragma unroll
        for (int m = 0; m < 8; m += 2) st_dev(ro, ob + (n * 64 + m / 2) * 1024, pack_pair(acc[m][n], acc[m + 1][n]));
      publish(FLAG(FL_AB + pn1 * 8 + pm1), tag);
      STAMP(1, 3);
    }
    {
      Pipe<GY> p; p.lds = ldsb; p.wid = wid;
      p.xl.rs = mkrsrc(wshare + (size_t)wid * 8 * S_, 32768); p.xl.step = 16384; p.xl.soff = 0u - (unsigned)(YS0 + kb % YSN) * 16384u; p.xl.voff = fq * 4096 + fr * 128 + 2048;
      p.A.init_rowk(wsm, S_ * S_ * 2, S_, pm * 256, 0, wid, lane);
      p.B.init_frag(ws + WS_AB, S_ * 2 * D_ * 2, 64, half * D_ + pn * 128, 0, wid, lane);
      p.prologue<1, false>();
      await(FLAG(FL_AB + (half * 8 + pn) * 8), 8, tag);
      STAMP(1, 5);
      p.prologue<2, true>();
      gemm_loop<GY, 32>(p, lane, acc, 6, ZeroInit(), NoHook());
#pragma unroll
      for (int i = 0; i < 16; ++i) st_dev(ry, lane * 16 + half * 65536 + wm * 32768 + (((i >> 3) * 2 + wn) * 8 + (i & 7)) * 1024, pack8(acc[i >> 1][(i & 1) * 2], acc[i >> 1][(i & 1) * 2 + 1]));
      asm volatile(".Lkm_endy:" ::: "memory");
      publish(FLAG(FL_Y + tile * 2 + half), tag);
      STAMP(1, 8);
    }
  } else {
    Pipe<GO> po; po.lds = ldsb; po.wid = wid;
    const size_t xo = (size_t)(pm * 256 + half * 128 + wm * 64 + (lane >> 3)) * D_ + pn * 128 + wn * 64 + (lane & 7) * 8;
    {
      const int c0 = pn1 * 224;
      Pipe<GH> p; p.lds = ldsb; p.wid = wid;
      p.xl.rs = mkrsrc((half ? ws_im : ws_re) + (size_t)(pm * 256 + pn * 32 + wid * 8) * S_, 32768); p.xl.step = 16384; p.xl.soff = 0u - (unsigned)(HS0 + kb % HSN) * 16384u; p.xl.voff = fq * 4096 + fr * 128 + 2048;
      static_for<0, 7>([&](auto N) { constexpr int n = N.value; gld2<32 + 2 * n>(b1 + c0 + (n < 6 ? wn * 96 + n * 16 : 192 + wn * 16) + fq * 4); });
      p.B.init_nmaj(w1, D_ * F_ * 2, F_, c0, 0, wid, lane);
      p.prologue<2, false>();
      layernorm(std::integral_constant<int, 21>{});
      p.A.init_frag(ws + WS_XA, S_ * D_ * 2, 32, pm1 * 256, 0, wid, lane);
      await(FLAG(FL_X + pm1 * 32), 32, tag);
      STAMP(1, 11);
      p.prologue<1, true>();
      f32x4 acc[8][7];
      const u32x2 bias[7] = {ard2<32>(), ard2<34>(), ard2<36>(), ard2<38>(), ard2<40>(), ard2<42>(), ard2<44>()};
      gemm_loop<GH, 16>(p, lane, acc, 1, [&](int n) { return bf4(bias[n]); }, NoHook());
      flag_req<125>(FLAG(FL_W2 + lane)); flag_req<126>(FLAG(FL_W2 + 64 + lane));
      const __amdgpu_buffer_rsrc_t ro = mkrsrc(ws + WS_H, S_ * F_ * 2);
      const unsigned ob = (unsigned)((pm1 * 16 + wm * 8) * 112) * 1024u, o1 = ob + (2 * pn1 + wn) * 1024 + lo, o2 = ob + (32 + 5 * pn1 + 2 * wn) * 1024 + lo,
                     os = ob + (32 + 5 * pn1 + 4) * 1024 + (wn * 2 + (fq >> 1)) * 256 + fr * 16 + (fq & 1) * 8;
#pragma unroll
      for (int m = 0; m < 8; ++m) st_dev(ro, o1 + m * 112 * 1024, pack_pair(gelu4(acc[m][0]), gelu4(acc[m][1])));
      asm volatile("" ::: "memory");
#pragma unroll
      for (int m = 0; m < 8; ++m) {
        st_dev(ro, o2 + m * 112 * 1024, pack_pair(gelu4(acc[m][2]), gelu4(acc[m][3])));
        st_dev(ro, o2 + m * 112 * 1024 + 1024, pack_pair(gelu4(acc[m][4]), gelu4(acc[m][5])));
        const f32x4 g = gelu4(acc[m][6]); st_dev8(ro, os + m * 112 * 1024, (u32x2){cvt_pk_bf16(g[0], g[1]), cvt_pk_bf16(g[2], g[3])});
        if (m == EP1) {
          wait_vm<3 * (EP1 + 1)>(); bar(); signal(FLAG(FL_H + pm1 * 32 + pn1), tag);
          if (!__all(ard<125>() == tag && ard<126>() == tag)) { poll(FLAG(FL_W2), 64, tag); poll(FLAG(FL_W2 + 64), 64, tag); }
          po.B.init_frag(ws + WS_W2, F_ * D_ * 2, 112, pn * 128, 0, wid, lane);
          po.prologue<2, false>();
          po.A.init_frag(ws + WS_H, S_ * F_ * 2, 112, pm * 256 + half * 128, 0, wid, lane);
        }
        if (m == EP2 - 1) flag_req<125>(FLAG(FL_H + pm * 32 + (lane & 15)));
        if (m == EP2) {
          wait_vm<3>();
          if (!__all(ard<125>() == tag)) poll(FLAG(FL_H + pm * 32), 16, tag);
          asm volatile("" ::: "memory");
          STAMP(1, 5);
          po.prologue<1, true>();
        }
      }
      STAMP(1, 3);
    }
    f32x4 acc[4][4];
    unsigned* fh2 = FLAG(FL_H + pm * 32 + 16);
    unsigned* fy = FLAG(FL_Y + tile * 2);
    auto yreq = [&]() {
      static_for<0, 16>([&](auto I) { constexpr int i = I.value;
        bld4_dev<128 + 4 * i, (i & 3) * 1024>(ry, lane * 16, (i >> 3) * 65536 + half * 32768 + wid * 8192 + (i & 4) * 1024); });
    };
    gemm_loop<GO, 56>(po, lane, acc, 6, ZeroInit(), [&](int it) {
      if (it == 14) signal(FLAG(FL_H + pm1 * 32 + 16 + pn1), tag);
      if (it == 13) flag_req<125>(fh2 + (lane & 15) * FS);
      if (it == 12) { if (!__all(ard<125>() == tag)) poll(fh2, 16, tag); asm volatile("" ::: "memory"); }
      if (it == 2) { flag_req<125>(fy + (lane & 1) * FS); static_for<0, 4>([&](auto N) { gld2<80 + 2 * N.value>(b2 + pn * 128 + wn * 64 + N.value * 16 + fq * 4); }); }
    });
    if (!__all(ard<125>() == tag)) poll(fy, 2, tag);
    asm volatile("" ::: "memory");
    yreq();
    static_for<0, 8>([&](auto J) { constexpr int j = J.value; gld4_nt<16 + 4 * (j & 3) + 32 * (j >> 2), 0>(x + xo + (size_t)j * 8 * D_); });
    STAMP(1, 8);
    const u32x2 bias[4] = {ard2<80>(), ard2<82>(), ard2<84>(), ard2<86>()};
#pragma unroll
    for (int i = 0; i < 16; ++i) { const f32x4 t = acc[i >> 2][i & 3] + bf4(bias[i & 3]);
      const f32x2 a = silu2((f32x2){t[0], t[1]}), b = silu2((f32x2){t[2], t[3]}); acc[i >> 2][i & 3] = (f32x4){a.x, a.y, b.x, b.y}; pinacc(acc[i >> 2][i & 3]); }
    wait_vm<0>(); bar();
    const u32x4 xr[8] = {ard4<16>(), ard4<20>(), ard4<24>(), ard4<28>(), ard4<48>(), ard4<52>(), ard4<56>(), ard4<60>()};
    u32x4 yr[8], yi[8];
    static_for<0, 8>([&](auto I) { yr[I.value] = ard4<128 + 4 * I.value>(); yi[I.value] = ard4<160 + 4 * I.value>(); });
    LAS unsigned char* tl = (LAS unsigned char*)shm + wid * (64 * 144);
#pragma unroll
    for (int i = 0; i < 8; ++i) {
      unsigned r[4];
#pragma unroll
      for (int e = 0; e < 4; ++e) { const f32x4 g = acc[i >> 1][(i & 1) * 2 + (e >> 1)];
        r[e] = cvt_pk_bf16((bf_lo(yr[i][e]) - bf_lo(yi[i][e])) * g[(e & 1) * 2], (bf_hi(yr[i][e]) - bf_hi(yi[i][e])) * g[(e & 1) * 2 + 1]); }
      LAS unsigned char* q = tl + ((i >> 1) * 16 + fr) * 144 + (i & 1) * 64 + fq * 8;
      *(LAS u32x2*)q = (u32x2){r[0], r[1]}; *(LAS u32x2*)(q + 32) = (u32x2){r[2], r[3]};
    }
    wait_lgkm0();
#pragma unroll
    for (int j = 0; j < 8; ++j) {
      const u32x4 r = *(const LAS u32x4*)(tl + (j * 8 + (lane >> 3)) * 144 + (lane & 7) * 16);
      u32x4 o;
#pragma unroll
      for (int e = 0; e < 4; ++e) o[e] = cvt_pk_bf16(bf_lo(xr[j][e]) + bf_lo(r[e]), bf_hi(xr[j][e]) + bf_hi(r[e]));
      __builtin_nontemporal_store(o, (u32x4*)(out + xo + (size_t)j * 8 * D_));
    }
    asm volatile(".Lkm_endo:" ::: "memory");
    if (blockIdx.x == 255) {
      poll(FLAG(FL_X + wid * 64), 64, tag);
      asm volatile("" ::: "memory"); bar();
      signal(FLAG(FL_E), tag);
    }
    STAMP(1, 9);
  }
}

__global__ void k_rd(const u32x4* p, int n, unsigned* sink) { u32x4 a = {0,0,0,0}; for (int i = blockIdx.x * 256 + threadIdx.x; i < n; i += gridDim.x * 256) a ^= p[i]; if (a[0] + a[1] + a[2] + a[3] == 0x12345677u) *sink = 1; }
__global__ void k_rd7(const u32x4* p0, const u32x4* p1, const u32x4* p2, const u32x4* p3, const u32x4* p4, const u32x4* p5, const u32x4* p6, unsigned* sink) {
  const u32x4* ps[7] = {p0, p1, p2, p3, p4, p5, p6}; const int ns[7] = {262144, 524288, 524288, 131072, 131072, 458752, 458752}; u32x4 a = {0,0,0,0};
#pragma unroll
  for (int k = 0; k < 7; ++k) for (int i = blockIdx.x * 256 + threadIdx.x; i < ns[k]; i += gridDim.x * 256) a ^= ps[k][i];
  if (a[0] + a[1] + a[2] + a[3] == 0x12345677u) *sink = 1; }
__global__ void k_wr(u32x4* p, int n) { for (int i = blockIdx.x * 256 + threadIdx.x; i < n; i += gridDim.x * 256) p[i] = (u32x4){1u, 2u, 3u, (unsigned)i}; }
extern "C" void kernel_launch(void* const* d_in, const int* in_sizes, int n_in, void* d_out, int out_size, void* d_ws, size_t ws_size, hipStream_t stream) {
  const u16* x = (const u16*)d_in[0]; const u16* ws_re = (const u16*)d_in[1]; const u16* ws_im = (const u16*)d_in[2];
  const u16* wd_re = (const u16*)d_in[3]; const u16* wd_im = (const u16*)d_in[4];
  const u16* w1 = (const u16*)d_in[6]; const u16* b1 = (const u16*)d_in[7]; const u16* w2 = (const u16*)d_in[8]; const u16* b2 = (const u16*)d_in[9];
  unsigned char* ws = (unsigned char*)d_ws;
  (void)hipFuncSetAttribute((const void*)k_main, hipFuncAttributeMaxDynamicSharedMemorySize, LDSM);
  k_rd7<<<1024, 256, 0, stream>>>((const u32x4*)x, (const u32x4*)ws_re, (const u32x4*)ws_im, (const u32x4*)wd_re, (const u32x4*)wd_im, (const u32x4*)w1, (const u32x4*)w2, (unsigned*)(ws + (200u << 20)));
  k_main<<<256, 256, LDSM, stream>>>(x, ws_re, ws_im, wd_re, wd_im, w1, b1, w2, b2, ws, (u16*)d_out);
}
```

```cpp
#include <hip/hip_runtime.h>
#include <hip/hip_bf16.h>
#include <type_traits>
#include <utility>

#define LAS __attribute__((address_space(3)))
#define DI __device__ __forceinline__
typedef unsigned short u16;
typedef short bf16x4 __attribute__((ext_vector_type(4)));
typedef float f32x4 __attribute__((ext_vector_type(4)));
typedef float f32x2 __attribute__((ext_vector_type(2)));
typedef unsigned u32x4 __attribute__((ext_vector_type(4)));
typedef unsigned u32x2 __attribute__((ext_vector_type(2)));

constexpr int S_ = 2048, D_ = 1024, F_ = 3584;
constexpr size_t WS_FLAG = 0;
constexpr int FS = 16, FL_E = 0, FL_X = 32, FL_AB = FL_X + 256, FL_H = FL_AB + 128, FL_W2 = FL_H + 256, FL_Y = FL_W2 + 128;
constexpr size_t WS_XA = 262144;
constexpr size_t WS_AB = WS_XA + (size_t)S_ * D_ * 2;
constexpr size_t WS_H = WS_AB + (size_t)S_ * 2 * D_ * 2;
constexpr size_t WS_W2 = WS_H + (size_t)S_ * F_ * 2;
constexpr size_t WS_PY = WS_W2 + (size_t)F_ * D_ * 2;
constexpr unsigned OOB = 0x40000000u;
constexpr int SPIN_CAP = 1 << 18;

template <int N> DI void wait_vm() { asm volatile("s_waitcnt vmcnt(%0)" ::"n"(N) : "memory"); }
DI void wait_lgkm0() { asm volatile("s_waitcnt lgkmcnt(0)" ::: "memory"); }
template <int N> DI void wait_lgkm() { asm volatile("s_waitcnt lgkmcnt(%0)" ::"n"(N > 15 ? 15 : N) : "memory"); }
DI void bar() { asm volatile("s_barrier" ::: "memory"); }
DI unsigned cvt_pk_bf16(float lo, float hi) {
  typedef __bf16 bf16x2 __attribute__((ext_vector_type(2)));
  f32x2 v = {lo, hi}; bf16x2 b = __builtin_convertvector(v, bf16x2); return __builtin_bit_cast(unsigned, b);
}
DI float bf_lo(unsigned w) { return __uint_as_float(w << 16); }
DI float bf_hi(unsigned w) { return __uint_as_float(w & 0xffff0000u); }
DI float rbf(float v) { return bf_lo(cvt_pk_bf16(v, v)); }
template <int I, int N, class F> DI void static_for(F&& f) { if constexpr (I < N) { f(std::integral_constant<int, I>{}); static_for<I + 1, N>(f); } }

#define ACLOB "a0","a1","a2","a3","a4","a5","a6","a7","a8","a9","a10","a11","a12","a13","a14","a15","a16","a17","a18","a19","a20","a21","a22","a23","a24","a25","a26","a27","a28","a29","a30","a31","a32","a33","a34","a35","a36","a37","a38","a39","a40","a41","a42","a43","a44","a45","a46","a47","a48","a49","a50","a51","a52","a53","a54","a55","a56","a57","a58","a59","a60","a61","a62","a63","a64","a65","a66","a67","a68","a69","a70","a71","a72","a73","a74","a75","a76","a77","a78","a79","a80","a81","a82","a83","a84","a85","a86","a87","a88","a89","a90","a91","a92","a93","a94","a95","a96","a97","a98","a99","a100","a101","a102","a103","a104","a105","a106","a107","a108","a109","a110","a111","a112","a113","a114","a115","a116","a117","a118","a119","a120","a121","a122","a123","a124","a125","a126","a127","a128","a129","a130","a131","a132","a133","a134","a135","a136","a137","a138","a139","a140","a141","a142","a143","a144","a145","a146","a147","a148","a149","a150","a151","a152","a153","a154","a155","a156","a157","a158","a159","a160","a161","a162","a163","a164","a165","a166","a167","a168","a169","a170","a171","a172","a173","a174","a175","a176","a177","a178","a179","a180","a181","a182","a183","a184","a185","a186","a187","a188","a189","a190","a191"
template <int RA, int RB> DI void mfma16(f32x4& acc) { asm volatile("v_mfma_f32_16x16x32_bf16 %0, a[%c1:%c2], a[%c3:%c4], %0" : "+v"(acc) : "n"(RA), "n"(RA + 3), "n"(RB), "n"(RB + 3)); }
template <int RD, int OFF> DI void ds_r128(unsigned addr) { asm volatile("ds_read_b128 a[%c1:%c2], %0 offset:%c3" ::"v"(addr), "n"(RD), "n"(RD + 3), "n"(OFF) : "memory"); }
template <int RD, int OFF> DI void ds_rtr(unsigned addr) { asm volatile("ds_read_b64_tr_b16 a[%c1:%c2], %0 offset:%c3" ::"v"(addr), "n"(RD), "n"(RD + 1), "n"(OFF) : "memory"); }
DI void wait_frags() { asm volatile("s_waitcnt lgkmcnt(0)" ::: "memory", ACLOB); }
template <int OFF, int POL> DI void dma16(int voff, __amdgpu_buffer_rsrc_t r, unsigned soff) {
  if constexpr (POL == 0) asm volatile("buffer_load_dwordx4 %0, %1, %2 offen offset:%3 lds" ::"v"(voff), "s"(r), "s"(soff), "n"(OFF) : "memory");
  if constexpr (POL == 1) asm volatile("buffer_load_dwordx4 %0, %1, %2 offen offset:%3 sc1 lds" ::"v"(voff), "s"(r), "s"(soff), "n"(OFF) : "memory");
  if constexpr (POL == 2) asm volatile("buffer_load_dwordx4 %0, %1, %2 offen offset:%3 nt lds" ::"v"(voff), "s"(r), "s"(soff), "n"(OFF) : "memory");
}
DI void pinacc(f32x4& v) { asm volatile("" : "+v"(v)); }
DI __amdgpu_buffer_rsrc_t mkrsrc(const void* p, unsigned bytes) { return __builtin_amdgcn_make_buffer_rsrc((void*)p, 0, bytes, 0x00020000); }

enum { ROWK = 0, FRAG = 1, NMAJ = 2 };

template <int KIND, int R, int POL> struct Stream {
  static constexpr bool ODD = KIND == NMAJ && R == 224;
  static constexpr int P = R / 32, NG = (P + 3) / 4, NV = ODD ? 7 : (KIND == FRAG ? 2 : 4);
  __amdgpu_buffer_rsrc_t rs;
  int voff[NV];
  unsigned s0[NG], ks;
  unsigned ml[NG];
  static DI int gpiece(int g, int wid) { if constexpr (ODD) return wid * 7 + 4 * g; else return 16 * g + 4 * wid; }
  DI void init_rowk(const void* p, unsigned bytes, int ld, int r0, int k0, int wid, int lane) {
    rs = mkrsrc(p, bytes);
#pragma unroll
    for (int j = 0; j < 4; ++j) voff[j] = ((lane >> 3) + 8 * j) * ld * 2 + (((lane & 7) ^ ((4 * (j & 1) + (lane >> 4)) & 7)) * 16) - j * 1024;
#pragma unroll
    for (int g = 0; g < NG; ++g) s0[g] = (unsigned)((r0 + 8 * gpiece(g, wid)) * ld + k0) * 2u;
    ks = 128u;
  }
  DI void init_frag(const void* p, unsigned bytes, int kbn, int r0, int k0, int wid, int lane) {
    rs = mkrsrc(p, bytes);
    voff[0] = lane * 16; voff[1] = lane * 16 + kbn * 1024 - 2048;
#pragma unroll
    for (int g = 0; g < NG; ++g) s0[g] = (unsigned)((r0 / 16 + gpiece(g, wid) / 2) * kbn + k0 / 32) * 1024u;
    ks = 2048u;
  }
  DI void init_nmaj(const void* p, unsigned bytes, int ld, int n0, int k0, int wid, int lane) {
    rs = mkrsrc(p, bytes);
    if constexpr (ODD) {
#pragma unroll
      for (int j = 0; j < 7; ++j) { const int L = (wid * 7 + j) * 64 + lane, kk = L / 28, pos = L % 28;
        voff[j] = kk * ld * 2 + (pos ^ (((kk >> 3) & 1) << 1)) * 16 - (j & 3) * 1024; }
      s0[0] = s0[1] = (unsigned)(k0 * ld + n0) * 2u;
    } else {
      static_assert(R == 128 || ODD, "");
#pragma unroll
      for (int j = 0; j < 4; ++j) { const int kk = 4 * (4 * wid + j) + (lane >> 4), g = (kk & 3) | (((kk >> 3) & 1) << 2);
        voff[j] = (4 * j + (lane >> 4)) * ld * 2 + (((lane & 15) ^ (g << 1)) * 16) - j * 1024; }
      s0[0] = (unsigned)((k0 + 16 * wid) * ld + n0) * 2u;
    }
    ks = (unsigned)(64 * ld) * 2u;
  }
  DI void init_lds(unsigned base, int wid) {
#pragma unroll
    for (int g = 0; g < NG; ++g) ml[g] = base + (unsigned)gpiece(g, wid) * 1024u;
  }
  template <int I, int SO> DI void m0() const { asm volatile("s_add_u32 m0, %0, %1" ::"s"(ml[I / 4]), "n"(SO) : "memory", "scc"); }
  template <int I> DI void issue() const {
    constexpr int g = I / 4, j = I % 4, vi = ODD ? I : (KIND == FRAG ? (j >> 1) : j);
    dma16<j * 1024, POL>(voff[vi], rs, s0[g]);
  }
  unsigned se[NG];
  DI void init_end(int nt) {
#pragma unroll
    for (int g = 0; g < NG; ++g) se[g] = s0[g] + nt * ks;
  }
  template <int I> DI void advance() { asm volatile("s_add_u32 %0, %0, %1\n\ts_cmp_eq_u32 %0, %2\n\ts_cselect_b32 %0, 2.0, %0" : "+s"(s0[I / 4]) : "s"(ks), "s"(se[I / 4]) : "scc"); }
};

template <int KIND, int R, int T, int NS, int STAGE> struct Reader {
  static constexpr int NA = KIND == FRAG ? 1 : (KIND == ROWK ? 2 : 3);
  static constexpr int SPAN = KIND == NMAJ ? 64 * R * 2 : T * 2048, SPW = (65536 - SPAN) / STAGE + 1, NW = (NS + SPW - 1) / SPW;
  unsigned a[NW][NA];
  DI void init(unsigned base, int t0, int lane) {
    const int fr = lane & 15, fq = lane >> 4;
    if constexpr (KIND == ROWK) { a[0][0] = base + (t0 * 16 + fr) * 128 + ((fq ^ (fr >> 1)) * 16); a[0][1] = base + (t0 * 16 + fr) * 128 + (((4 + fq) ^ (fr >> 1)) * 16); }
    else if constexpr (KIND == FRAG) a[0][0] = base + t0 * 2048 + lane * 16;
    else { static_assert(KIND != NMAJ || R == 224, "");
      const int q = fr >> 2, p = lane & 3, wn = t0 / 7; const unsigned b = base + (8 * fq + q) * 448 + 8 * p;
      a[0][0] = b + 32 * ((6 * wn) ^ (fq & 1)); a[0][1] = b + 32 * ((6 * wn + 1) ^ (fq & 1)); a[0][2] = b + 32 * ((12 + wn) ^ (fq & 1)); }
#pragma unroll
    for (int w = 1; w < NW; ++w)
#pragma unroll
      for (int n = 0; n < NA; ++n) { a[w][n] = a[0][n] + w * SPW * STAGE; asm volatile("" : "+v"(a[w][n])); }
  }
  template <int ST, int KS, int I, int RB> DI void rd() const {
    constexpr int w = ST / SPW, so = (ST % SPW) * STAGE;
    if constexpr (KIND == ROWK) ds_r128<RB + 4 * I, so + I * 2048>(a[w][KS]);
    else if constexpr (KIND == FRAG) ds_r128<RB + 4 * I, so + I * 2048 + KS * 1024>(a[w][0]);
    else { constexpr int n = I / 2, h = I % 2; ds_rtr<RB + 4 * n + 2 * h, so + (32 * KS + 4 * h) * (R * 2) + (n < 6 ? 32 * (n & ~1) : 0)>(a[w][n < 6 ? (n & 1) : 2]); }
  }
};

template <int NTILE_, int BM_, int BN_, int NS_, int KA_, int KB_, int PLA_, int PLB_, bool TR_, int NX_, bool LAZY_, int RSP_> struct Geo {
  static constexpr bool LAZY = LAZY_; static constexpr int NTILE = NTILE_, NX = NX_, PLA = PLA_, PLB = PLB_;
  static constexpr int BM = BM_, BN = BN_, NS = NS_, KA = KA_, KB = KB_, MT = BM / 32, NT = BN / 32, NM = MT * NT;
  static constexpr bool TR = TR_;
  static constexpr int PA = BM / 32, PB = BN / 32, ND = PA + PB, STAGE = (BM + BN) * 128;
  static constexpr int DEPTH = LAZY ? NS - 1 : NS;
  static constexpr int H = (DEPTH == 2) ? ND : ND / 2;
  static constexpr int NIA = KA == NMAJ ? 2 * MT : MT, NIB = KB == NMAJ ? 2 * NT : NT, NR = NIA + NIB, RSPAN = RSP_;
  static constexpr int rgap(int r) { return (r * RSPAN) / NR; }
  static constexpr int rbefore(int i) { int c = 0; for (int r = 0; r < NR; ++r) c += rgap(r) < i; return c; }
  typedef Reader<KA, BM, MT, NS, STAGE> RA; typedef Reader<KB, BN, NT, NS, STAGE> RB;
};
struct Side { __amdgpu_buffer_rsrc_t rs; unsigned voff, step, soff; };
template <class G> struct Pipe {
  Stream<G::KA, G::BM, G::PLA> A; Stream<G::KB, G::BN, G::PLB> B;
  Side xl, x2;
  unsigned lds; int wid;
  DI void side() {
    if constexpr (G::NX > 0) asm volatile("buffer_load_dword a124, %0, %1, %2 offen" ::"v"(xl.voff), "s"(xl.rs), "s"(xl.soff) : "memory");
    if constexpr (G::NX > 1) asm volatile("buffer_load_dword a124, %0, %1, %2 offen" ::"v"(x2.voff), "s"(x2.rs), "s"(x2.soff) : "memory"); }
  DI void side_adv() {
    if constexpr (G::NX > 0) asm volatile("s_add_u32 %0, %0, %1" : "+s"(xl.soff) : "s"(xl.step) : "scc");
    if constexpr (G::NX > 1) asm volatile("s_add_u32 %0, %0, %1" : "+s"(x2.soff) : "s"(x2.step) : "scc"); }
  static constexpr bool gstart(int i) { return i < G::PA ? (i % 4 == 0) : ((i - G::PA) % 4 == 0); }
  static constexpr bool gend(int i) { return i == G::PA - 1 || i == G::ND - 1 || gstart(i + 1); }
  template <int I, int ST> DI void m0() const { if constexpr (I < G::PA) A.template m0<I, ST * G::STAGE>(); else B.template m0<I - G::PA, ST * G::STAGE>(); }
  template <int I> DI void issue() const { if constexpr (I < G::PA) A.template issue<I>(); else B.template issue<I - G::PA>(); }
  template <int I> DI void advance() { if constexpr (I < G::PA) A.template advance<I>(); else B.template advance<I - G::PA>(); }
  template <int MASK, bool LAST> DI void prologue() {
    if constexpr (MASK & 1) { A.init_lds(lds, wid); A.init_end(G::NTILE); }
    if constexpr (MASK & 2) { B.init_lds(lds + G::BM * 128, wid); B.init_end(G::NTILE); }
    asm volatile("s_nop 4" ::: "memory", ACLOB);
    static_for<0, G::DEPTH>([&](auto Ss) {
      if constexpr (LAST && Ss.value >= 2) { side(); side_adv(); }
      static_for<0, (Ss.value == G::DEPTH - 1 ? G::H : G::ND)>([&](auto Dd) { constexpr int d = Dd.value;
        if constexpr ((MASK >> (d < G::PA ? 0 : 1)) & 1) {
          if constexpr (gstart(d)) { m0<d, Ss.value>(); asm volatile("s_nop 0"); }
          issue<d>();
          if constexpr (gend(d)) advance<d>(); }
        else if constexpr (LAST && Ss.value >= 1) asm volatile("buffer_load_dword a124, off, %0, %1" ::"s"(A.rs), "s"(OOB) : "memory"); }); });
    if constexpr (LAST && G::H < G::ND) if constexpr (!gstart(G::H)) m0<G::H, G::DEPTH - 1>();
  }
};

constexpr int dgap(int d, int ndp, int nm, int w) { return 1 + (d * (nm - 2) + w * (nm - 2) / 4) / ndp; }
constexpr int dat(int i, int ndp, int nm, int w) { for (int d = 0; d < ndp; ++d) if (dgap(d, ndp, nm, w) == i) return d; return -1; }
template <class G, int W, int CUR, int RST, int RKS, int D0, int D1, int DST, int I>
DI void gap(Pipe<G>& p, const typename G::RA& ra, const typename G::RB& rb, f32x4 (&acc)[G::MT][G::NT]) {
  constexpr int CA = 64 * CUR, CB = CA + 32, NA = 64 * (CUR ^ 1), NB = NA + 32;
  constexpr int NM = G::NM, NIB = G::NIB, NR = G::NR, NDP = D1 - D0, m = I / G::NT, n0 = I % G::NT, n = (m & 1) ? G::NT - 1 - n0 : n0;
  constexpr int d = dat(I, NDP, NM, W), dp = dat(I - 1, NDP, NM, W);
  if constexpr (n0 == 0 && m % 2 == 0 && (G::LAZY || CUR == 0)) wait_lgkm<(NR - 1 - (NIB + m + 1)) + G::rbefore(I)>();
  if constexpr (d >= 0) if constexpr (Pipe<G>::gstart(D0 + d)) p.template m0<D0 + d, DST>();
  if constexpr (G::TR) mfma16<CB + 4 * n, CA + 4 * m>(acc[m][n]); else mfma16<CA + 4 * m, CB + 4 * n>(acc[m][n]);
  if constexpr (I == 0 && CUR == (G::DEPTH == 2 ? 0 : 1)) p.side();
  if constexpr (I == NM - 1 && CUR == (G::DEPTH == 2 ? 0 : 1)) p.side_adv();
  if constexpr (dp >= 0) if constexpr (Pipe<G>::gend(D0 + dp)) p.template advance<D0 + dp>();
  if constexpr (d >= 0) p.template issue<D0 + d>();
  static_for<G::rbefore(I), G::rbefore(I + 1)>([&](auto Rr) { constexpr int r = Rr.value;
    if constexpr (r == 0) ra.template rd<RST, RKS, 0, NA>();
    else if constexpr (r <= NIB) rb.template rd<RST, RKS, r - 1, NB>();
    else ra.template rd<RST, RKS, r - NIB, NA>(); });
}
template <class G, int W, int CUR, int RST, int RKS, int D0, int D1, int DST, int... Is>
DI void phase_(Pipe<G>& p, const typename G::RA& ra, const typename G::RB& rb, f32x4 (&acc)[G::MT][G::NT], std::integer_sequence<int, Is...>) {
  (gap<G, W, CUR, RST, RKS, D0, D1, DST, Is>(p, ra, rb, acc), ...);
}
template <class G, int W, int ST>
DI void ktile(Pipe<G>& p, const typename G::RA& ra, const typename G::RB& rb, f32x4 (&acc)[G::MT][G::NT]) {
  constexpr int NS = G::NS, ND = G::ND, H = G::H, DP = G::DEPTH;
  phase_<G, W, 0, ST, 1, H, ND, (ST + DP - 1) % NS>(p, ra, rb, acc, std::make_integer_sequence<int, G::NM>{});
  if constexpr (G::LAZY) asm volatile("s_waitcnt vmcnt(%0)\n\ts_barrier" ::"n"((DP - 2) * (ND + G::NX)) : "memory", ACLOB);
  else asm volatile("s_waitcnt vmcnt(%0) lgkmcnt(0)\n\ts_barrier" ::"n"(DP == 2 ? G::NX : (DP - 2) * (ND + G::NX)) : "memory", ACLOB);
  phase_<G, W, 1, (ST + 1) % NS, 0, 0, H, (ST + DP) % NS>(p, ra, rb, acc, std::make_integer_sequence<int, G::NM>{});
}
template <class G, int W, int NTILE, class Hook>
DI void gemm_body(Pipe<G>& p, const typename G::RA& ra, const typename G::RB& rb, f32x4 (&acc)[G::MT][G::NT], int on, Hook hook) {
#pragma nounroll
  for (int it = on * (NTILE / G::NS); it > 0; --it) { static_for<0, G::NS>([&](auto Ss) { ktile<G, W, Ss.value>(p, ra, rb, acc); });
    hook(it);
  }
  if constexpr (NTILE % G::NS != 0) {
    asm volatile("" : "+s"(on));
#pragma nounroll
    for (int it = on; it > 0; --it) static_for<0, NTILE % G::NS>([&](auto Ss) { ktile<G, W, Ss.value>(p, ra, rb, acc); });
  }
}
template <class G, int NTILE, class Init, class Hook>
DI void gemm_loop(Pipe<G>& p, int lane, f32x4 (&acc)[G::MT][G::NT], Init init, Hook hook) {
  constexpr int ND = G::ND, H = G::H, DP = G::DEPTH;
  typename G::RA ra; typename G::RB rb;
  ra.init(p.lds, (p.wid >> 1) * G::MT, lane); rb.init(p.lds + G::BM * 128, (p.wid & 1) * G::NT, lane);
#pragma unroll
  for (int n = 0; n < G::NT; ++n) { const f32x4 v = init(n);
#pragma unroll
    for (int m = 0; m < G::MT; ++m) { acc[m][n] = v; pinacc(acc[m][n]); } }
  wait_vm<(DP - 2) * (ND + G::NX) + H>();
  bar();
  static_for<0, G::NIA>([&](auto I) { ra.template rd<0, 0, I.value, 0>(); });
  static_for<0, G::NIB>([&](auto I) { rb.template rd<0, 0, I.value, 32>(); });
  wait_frags();
  asm volatile("s_nop 1");
  gemm_body<G, 0, NTILE>(p, ra, rb, acc, 1, hook);
  asm volatile("s_nop 7\n\ts_nop 7\n\ts_waitcnt vmcnt(0) lgkmcnt(0)" ::: "memory", ACLOB);
#pragma unroll
  for (int m = 0; m < G::MT; ++m)
#pragma unroll
    for (int n = 0; n < G::NT; ++n) pinacc(acc[m][n]);
}

DI void touch(const void* p) { asm volatile("global_load_dword a124, %0, off" ::"v"(p) : "memory"); }
template <int R, int OFF> DI void gld4(const void* p) { asm volatile("global_load_dwordx4 a[%c1:%c2], %0, off offset:%c3" ::"v"(p), "n"(R), "n"(R + 3), "n"(OFF) : "memory"); }
template <int R> DI void gld2(const void* p) { asm volatile("global_load_dwordx2 a[%c1:%c2], %0, off" ::"v"(p), "n"(R), "n"(R + 1) : "memory"); }
template <int R, int OFF> DI void bld4_dev(__amdgpu_buffer_rsrc_t r, unsigned voff, unsigned soff) { asm volatile("buffer_load_dwordx4 a[%c3:%c4], %0, %1, %2 offen offset:%c5 sc1" ::"v"(voff), "s"(r), "s"(soff), "n"(R), "n"(R + 3), "n"(OFF) : "memory"); }
template <int R> DI void flag_req(const unsigned* p) { asm volatile("global_load_dword a[%c1], %0, off sc1" ::"v"(p), "n"(R) : "memory"); }
template <int R> DI unsigned ard() { unsigned r; asm volatile("v_accvgpr_read_b32 %0, a[%c1]" : "=v"(r) : "n"(R)); return r; }
template <int R> DI u32x4 ard4() { return (u32x4){ard<R>(), ard<R + 1>(), ard<R + 2>(), ard<R + 3>()}; }
template <int R> DI u32x2 ard2() { return (u32x2){ard<R>(), ard<R + 1>()}; }
template <int CTRL> DI float dpp_add(float a) { return a + __builtin_bit_cast(float, __builtin_amdgcn_update_dpp(0, __builtin_bit_cast(int, a), CTRL, 0xf, 0xf, true)); }
template <int N> DI void wave_sum(float (&s)[N]) {
#pragma unroll
  for (int r = 0; r < N; ++r) s[r] = dpp_add<0xB1>(s[r]);
#pragma unroll
  for (int r = 0; r < N; ++r) s[r] = dpp_add<0x4E>(s[r]);
#pragma unroll
  for (int r = 0; r < N; ++r) s[r] = dpp_add<0x141>(s[r]);
#pragma unroll
  for (int r = 0; r < N; ++r) s[r] = dpp_add<0x140>(s[r]);
#pragma unroll
  for (int r = 0; r < N; ++r) { const auto p = __builtin_amdgcn_permlane16_swap(__float_as_uint(s[r]), __float_as_uint(s[r]), false, false); s[r] = __uint_as_float(p[0]) + __uint_as_float(p[1]); }
#pragma unroll
  for (int r = 0; r < N; ++r) { const auto p = __builtin_amdgcn_permlane32_swap(__float_as_uint(s[r]), __float_as_uint(s[r]), false, false); s[r] = __uint_as_float(p[0]) + __uint_as_float(p[1]); }
}

DI f32x2 sigm_mul(f32x2 x, f32x2 arg) {
  f32x2 e; e.x = __builtin_amdgcn_exp2f(arg.x); e.y = __builtin_amdgcn_exp2f(arg.y);
  const f32x2 d = e + 1.0f;
  f32x2 r; r.x = __builtin_amdgcn_rcpf(d.x); r.y = __builtin_amdgcn_rcpf(d.y);
  return x * r;
}
DI f32x2 gelu2(f32x2 x) {
  const float k0 = -2.0f * 0.7978845608028654f * 1.4426950408889634f, k1 = k0 * 0.044715f;
  return sigm_mul(x, x * ((x * x) * k1 + k0));
}
DI f32x2 silu2(f32x2 x) { return sigm_mul(x, x * -1.4426950408889634f); }
DI f32x4 gelu4(f32x4 t) { const f32x2 a = gelu2((f32x2){t[0], t[1]}), b = gelu2((f32x2){t[2], t[3]}); return (f32x4){a.x, a.y, b.x, b.y}; }
DI f32x4 bf4(u32x2 w) { return (f32x4){bf_lo(w[0]), bf_hi(w[0]), bf_lo(w[1]), bf_hi(w[1])}; }

typedef Geo<16, 256, 128, 3, FRAG, ROWK, 1, 0, false, 1, false, 24> GAB;
typedef Geo<32, 256, 128, 3, ROWK, FRAG, 0, 1, true, 0, false, 24> GY;
typedef Geo<16, 256, 224, 2, FRAG, NMAJ, 1, 0, true, 1, false, 42> GH;
typedef Geo<56, 128, 128, 4, FRAG, FRAG, 1, 1, true, 0, false, 11> GO;
constexpr int EP1 = 1, EP2 = 5;
constexpr int LDSM = GY::NS * GY::STAGE > GO::NS * GO::STAGE ? GY::NS * GY::STAGE : GO::NS * GO::STAGE;

DI u32x4 pack_pair(const f32x4& a, const f32x4& b) {
  const unsigned a0 = cvt_pk_bf16(a[0], a[1]), a1 = cvt_pk_bf16(a[2], a[3]), b0 = cvt_pk_bf16(b[0], b[1]), b1 = cvt_pk_bf16(b[2], b[3]);
  const auto r0 = __builtin_amdgcn_permlane16_swap(a0, b0, false, false);
  const auto r1 = __builtin_amdgcn_permlane16_swap(a1, b1, false, false);
  return (u32x4){r0[0], r1[0], r0[1], r1[1]};
}
DI u32x4 pack8(const f32x4& a, const f32x4& b) { return (u32x4){cvt_pk_bf16(a[0], a[1]), cvt_pk_bf16(a[2], a[3]), cvt_pk_bf16(b[0], b[1]), cvt_pk_bf16(b[2], b[3])}; }
DI unsigned hpos(int t, int b) { return b == 0 ? 2 * t : (b == 3 ? 2 * t + 1 : 32 + 5 * t + (b < 3 ? b - 1 : b - 2)); }
struct NoHook { DI void operator()(int) const {} };
struct ZeroInit { DI f32x4 operator()(int) const { return (f32x4){0.f, 0.f, 0.f, 0.f}; } };

DI void st_dev(__amdgpu_buffer_rsrc_t r, unsigned off, u32x4 v) { __builtin_amdgcn_raw_buffer_store_b128(v, r, off, 0, 16); }
DI void st_dev8(__amdgpu_buffer_rsrc_t r, unsigned off, u32x2 v) { __builtin_amdgcn_raw_buffer_store_b64(v, r, off, 0, 16); }
DI void signal(unsigned* f, unsigned tag) { if (threadIdx.x == 0) __hip_atomic_store(f, tag, __ATOMIC_RELAXED, __HIP_MEMORY_SCOPE_AGENT); }
DI void publish(unsigned* f, unsigned tag) { wait_vm<0>(); bar(); signal(f, tag); }
DI void poll(unsigned* f, int n, unsigned tag) {
  unsigned* q = f + ((int)(threadIdx.x & 63) < n ? (threadIdx.x & 63) : 0) * FS;
#pragma nounroll
  for (int i = 0; i < SPIN_CAP && !__all(__hip_atomic_load(q, __ATOMIC_RELAXED, __HIP_MEMORY_SCOPE_AGENT) == tag); ++i) __builtin_amdgcn_s_sleep(1);
}
DI void await(unsigned* f, int n, unsigned tag) {
  if (threadIdx.x < 64) { poll(f, n, tag); wait_vm<0>(); }
  bar();
}

__global__ __launch_bounds__(256, 1) void k_main(const u16* __restrict__ x, const u16* __restrict__ ws_re, const u16* __restrict__ ws_im, const u16* __restrict__ wd_re, const u16* __restrict__ wd_im,
                                                 const u16* __restrict__ w1, const u16* __restrict__ b1, const u16* __restrict__ w2, const u16* __restrict__ b2, unsigned char* ws, u16* __restrict__ out) {
  extern __shared__ __attribute__((aligned(16))) unsigned char shm[];
  {
    const char* pc; unsigned len;
    asm volatile("s_getpc_b64 %0\n.Lkm_pc:\n\ts_mov_b32 %1, .Lkm_endy-.Lkm_pc\n\ts_max_i32 %1, %1, .Lkm_endo-.Lkm_pc" : "=s"(pc), "=s"(len) :: "scc");
    asm volatile("buffer_load_dword a124, %0, %1, 0 offen" ::"v"((((blockIdx.x >> 3) & 1) * 256 + threadIdx.x) * 128), "s"(mkrsrc(pc, len)) : "memory", ACLOB);
  }
  const int wid = __builtin_amdgcn_readfirstlane(threadIdx.x >> 6), lane = threadIdx.x & 63, fr = lane & 15, fq = lane >> 4, wm = wid >> 1, wn = wid & 1;
  const int xcd = blockIdx.x & 7, slot = blockIdx.x >> 3, chain = slot >> 4, s4 = slot & 15;
  const int pm1 = (xcd & 1) * 4 + (s4 & 3), pn1 = (xcd >> 1) * 4 + (s4 >> 2);
  const int half = xcd & 1, pm = (xcd >> 2) * 4 + (s4 & 3), pn = ((xcd >> 1) & 1) * 4 + (s4 >> 2), tile = pm * 8 + pn;
  const unsigned ldsb = (unsigned)(size_t)(LAS unsigned char*)shm;
  const int lo = ((fq & 1) * 2 + (fq >> 1)) * 256 + fr * 16;
  unsigned* const flag0 = (unsigned*)(ws + WS_FLAG);
#define FLAG(i) (flag0 + (i) * FS)
  const __amdgpu_buffer_rsrc_t ry = mkrsrc(ws + WS_PY + (size_t)tile * 131072, 131072);
  const int kb = xcd * 16 + s4;
  unsigned tag;
  {
    const u16* px = x + (size_t)(blockIdx.x * 8 + wid * 2) * D_ + lane * 8;
    asm volatile("global_load_dword a16, %0, off sc1" ::"v"(FLAG(FL_E)) : "memory");
    gld4<0, 0>(px); gld4<4, 1024>(px); gld4<8, 2048>(px); gld4<12, 3072>(px); }
  auto layernorm = [&](auto NY) {
    wait_vm<NY.value>();
    const u32x4 raw[4] = {ard4<0>(), ard4<4>(), ard4<8>(), ard4<12>()};
    const unsigned ev = ard<16>();
    f32x2 v[2][8]; float sm[2], mean[2];
#pragma unroll
    for (int i = 0; i < 16; ++i) v[i >> 3][i & 7] = (f32x2){bf_lo(raw[i >> 2][i & 3]), bf_hi(raw[i >> 2][i & 3])};
#pragma unroll
    for (int r = 0; r < 2; ++r) { const f32x2 a = ((v[r][0] + v[r][1]) + (v[r][2] + v[r][3])) + ((v[r][4] + v[r][5]) + (v[r][6] + v[r][7])); sm[r] = a.x + a.y; }
    wave_sum(sm);
#pragma unroll
    for (int r = 0; r < 2; ++r) { mean[r] = sm[r] * (1.0f / D_); f32x2 a = {0.f, 0.f};
#pragma unroll
      for (int e = 0; e < 8; ++e) { const f32x2 dd = v[r][e] - mean[r]; a += dd * dd; }
      sm[r] = a.x + a.y; }
    wave_sum(sm);
    LAS unsigned char* tl = (LAS unsigned char*)shm;
#pragma unroll
    for (int r = 0; r < 2; ++r) { const float rden = 1.0f / rbf(sqrtf(rbf(rbf(sm[r] * (1.0f / D_)) + 1e-5f))), mb = rbf(mean[r]);
#pragma unroll
      for (int j = 0; j < 2; ++j) { u32x4 o;
#pragma unroll
        for (int e = 0; e < 4; ++e) { const f32x2 d = v[r][4 * j + e] - mb; const unsigned db = cvt_pk_bf16(d.x, d.y); const f32x2 q = (f32x2){bf_lo(db), bf_hi(db)} * rden; o[e] = cvt_pk_bf16(q.x, q.y); }
        *(LAS u32x4*)(tl + (j * 64 + lane) * 128 + ((wid * 2 + r + lane) & 7) * 16) = o; } }
    tag = __builtin_amdgcn_readfirstlane(ev) + 1u;
    wait_lgkm0(); bar();
    const __amdgpu_buffer_rsrc_t rx = mkrsrc(ws + WS_XA + (size_t)(blockIdx.x >> 1) * 32768, 32768);
#pragma unroll
    for (int j = 0; j < 4; ++j) { const int ch = (wid * 4 + j) * 8 + (lane >> 3);
      st_dev(rx, ch * 256 + (blockIdx.x & 1) * 128 + (lane & 7) * 16, *(const LAS u32x4*)(tl + ch * 128 + ((lane + ch) & 7) * 16)); }
    publish(FLAG(FL_X + blockIdx.x), tag);
  };
  if (chain == 0) {
    const u16* wsm = half ? ws_im : ws_re;
    const u16* wshare = wsm + (size_t)(pm * 256 + pn * 32) * S_;
    f32x4 acc[8][4];
    {
      Pipe<GAB> p; p.lds = ldsb; p.wid = wid;
      p.B.init_rowk(pn1 < 8 ? wd_re : wd_im, D_ * D_ * 2, D_, (pn1 & 7) * 128, 0, wid, lane);
      p.prologue<2, false>();
      layernorm(std::integral_constant<int, 8>{});
      __builtin_amdgcn_s_sleep(16);
      {
        const int t = (s4 * 8 + xcd) * 256 + threadIdx.x;
        const int c = (s4 & 3) * 256 + threadIdx.x;
        touch((const char*)w1 + (size_t)t * 128);
        touch(t < 24576 ? (const char*)w1 + (size_t)(t + 32768) * 128 : (c < 512 ? (const char*)b1 + (c % 56) * 128 : (const char*)b2 + (c & 15) * 128));
        touch((const char*)(t < 16384 ? wd_re : wd_im) + (size_t)(t & 16383) * 128);
        const __amdgpu_buffer_rsrc_t rs = mkrsrc(w2 + (size_t)(kb < 112 ? kb : 0) * 32 * D_, kb < 112 ? 32 * D_ * 2 : 0);
        asm volatile("s_nop 4" ::: "memory");
        static_for<0, 16>([&](auto I) { constexpr int i = I.value;
          const int g = ((i >> 1) & 3) | ((wid & 1) << 2);
          asm volatile("s_add_u32 m0, %0, %1\n\ts_nop 0" ::"s"(ldsb + (1 + wm) * GAB::STAGE + wn * 16384), "n"(i * 1024) : "memory", "scc");
          dma16<0, 2>((lane ^ (g << 1)) * 16, rs, wid * 16384 + i * 1024); });
      }
      wait_vm<0>(); bar();
      if (kb < 112) {
        const int q = fr >> 2, gk = q | ((fq & 1) << 2);
        const __amdgpu_buffer_rsrc_t rw = mkrsrc(ws + WS_W2, F_ * D_ * 2);
        LAS unsigned char* src = (LAS unsigned char*)shm + (1 + (fq >> 1)) * GAB::STAGE + (8 * (fq & 1) + q) * 2048 + wid * 512 + 8 * (lane & 3);
        const unsigned kp = hpos(kb / 7, kb % 7);
#pragma unroll
        for (int n = 0; n < 16; ++n) {
          const bf16x4 l4 = __builtin_amdgcn_ds_read_tr16_b64_v4i16((LAS bf16x4*)(src + 32 * (n ^ gk)));
          const bf16x4 h4 = __builtin_amdgcn_ds_read_tr16_b64_v4i16((LAS bf16x4*)(src + 32 * (n ^ gk) + 8192));
          const u32x2 a = __builtin_bit_cast(u32x2, l4), b = __builtin_bit_cast(u32x2, h4);
          st_dev(rw, (unsigned)((wid * 16 + n) * 112 + kp) * 1024u + lane * 16, (u32x4){a[0], a[1], b[0], b[1]});
        }
        wait_lgkm0();
      }
      p.xl.rs = mkrsrc(wshare + (size_t)wid * 8 * S_, 32768); p.xl.step = 16384; p.xl.soff = 0u - (unsigned)(kb % 8) * 16384u; p.xl.voff = fq * 4096 + fr * 128;
      p.A.init_frag(ws + WS_XA, S_ * D_ * 2, 32, pm1 * 256, 0, wid, lane);
      if (wid != 0) wait_vm<0>();
      await(FLAG(FL_X + pm1 * 32), 32, tag);
      signal(FLAG(FL_W2 + kb), tag);
      p.prologue<1, true>();
      gemm_loop<GAB, 16>(p, lane, acc, ZeroInit(), NoHook());
      const __amdgpu_buffer_rsrc_t ro = mkrsrc(ws + WS_AB, S_ * 2 * D_ * 2);
      const unsigned ob = (unsigned)((pn1 * 8 + wn * 4) * 64 + pm1 * 8 + wm * 4) * 1024u + lo;
#pragma unroll
      for (int n = 0; n < 4; ++n)
#pragma unroll
        for (int m = 0; m < 8; m += 2) st_dev(ro, ob + (n * 64 + m / 2) * 1024, pack_pair(acc[m][n], acc[m + 1][n]));
      publish(FLAG(FL_AB + pn1 * 8 + pm1), tag);
    }
    {
      Pipe<GY> p; p.lds = ldsb; p.wid = wid;
      p.A.init_rowk(wsm, S_ * S_ * 2, S_, pm * 256, 0, wid, lane);
      p.B.init_frag(ws + WS_AB, S_ * 2 * D_ * 2, 64, half * D_ + pn * 128, 0, wid, lane);
      p.prologue<1, false>();
      await(FLAG(FL_AB + (half * 8 + pn) * 8), 8, tag);
      p.prologue<2, true>();
      gemm_loop<GY, 32>(p, lane, acc, ZeroInit(), NoHook());
#pragma unroll
      for (int i = 0; i < 16; ++i) st_dev(ry, lane * 16 + half * 65536 + wm * 32768 + (((i >> 3) * 2 + wn) * 8 + (i & 7)) * 1024, pack8(acc[i >> 1][(i & 1) * 2], acc[i >> 1][(i & 1) * 2 + 1]));
      asm volatile(".Lkm_endy:" ::: "memory");
      publish(FLAG(FL_Y + tile * 2 + half), tag);
    }
  } else {
    Pipe<GO> po; po.lds = ldsb; po.wid = wid;
    const size_t xo = (size_t)(pm * 256 + half * 128 + wm * 64 + (lane >> 3)) * D_ + pn * 128 + wn * 64 + (lane & 7) * 8;
    {
      const int c0 = pn1 * 224;
      Pipe<GH> p; p.lds = ldsb; p.wid = wid;
      p.xl.rs = mkrsrc((half ? ws_im : ws_re) + (size_t)(pm * 256 + pn * 32 + wid * 8) * S_, 32768); p.xl.step = 16384; p.xl.soff = 0u - (unsigned)(3 + kb % 8) * 16384u; p.xl.voff = fq * 4096 + fr * 128 + 2048;
      static_for<0, 7>([&](auto N) { constexpr int n = N.value; gld2<32 + 2 * n>(b1 + c0 + (n < 6 ? wn * 96 + n * 16 : 192 + wn * 16) + fq * 4); });
      p.B.init_nmaj(w1, D_ * F_ * 2, F_, c0, 0, wid, lane);
      p.prologue<2, false>();
      layernorm(std::integral_constant<int, 21>{});
      p.A.init_frag(ws + WS_XA, S_ * D_ * 2, 32, pm1 * 256, 0, wid, lane);
      await(FLAG(FL_X + pm1 * 32), 32, tag);
      p.prologue<1, true>();
      f32x4 acc[8][7];
      const u32x2 bias[7] = {ard2<32>(), ard2<34>(), ard2<36>(), ard2<38>(), ard2<40>(), ard2<42>(), ard2<44>()};
      gemm_loop<GH, 16>(p, lane, acc, [&](int n) { return bf4(bias[n]); }, NoHook());
      flag_req<125>(FLAG(FL_W2 + lane)); flag_req<126>(FLAG(FL_W2 + 64 + lane));
      const __amdgpu_buffer_rsrc_t ro = mkrsrc(ws + WS_H, S_ * F_ * 2);
      const unsigned ob = (unsigned)((pm1 * 16 + wm * 8) * 112) * 1024u, o1 = ob + (2 * pn1 + wn) * 1024 + lo, o2 = ob + (32 + 5 * pn1 + 2 * wn) * 1024 + lo,
                     os = ob + (32 + 5 * pn1 + 4) * 1024 + (wn * 2 + (fq >> 1)) * 256 + fr * 16 + (fq & 1) * 8;
#pragma unroll
      for (int m = 0; m < 8; ++m) st_dev(ro, o1 + m * 112 * 1024, pack_pair(gelu4(acc[m][0]), gelu4(acc[m][1])));
      asm volatile("" ::: "memory");
#pragma unroll
      for (int m = 0; m < 8; ++m) {
        st_dev(ro, o2 + m * 112 * 1024, pack_pair(gelu4(acc[m][2]), gelu4(acc[m][3])));
        st_dev(ro, o2 + m * 112 * 1024 + 1024, pack_pair(gelu4(acc[m][4]), gelu4(acc[m][5])));
        const f32x4 g = gelu4(acc[m][6]); st_dev8(ro, os + m * 112 * 1024, (u32x2){cvt_pk_bf16(g[0], g[1]), cvt_pk_bf16(g[2], g[3])});
        if (m == EP1) {
          wait_vm<3 * (EP1 + 1)>(); bar(); signal(FLAG(FL_H + pm1 * 32 + pn1), tag);
          if (!__all(ard<125>() == tag && ard<126>() == tag)) { poll(FLAG(FL_W2), 64, tag); poll(FLAG(FL_W2 + 64), 64, tag); }
          po.B.init_frag(ws + WS_W2, F_ * D_ * 2, 112, pn * 128, 0, wid, lane);
          po.prologue<2, false>();
          po.A.init_frag(ws + WS_H, S_ * F_ * 2, 112, pm * 256 + half * 128, 0, wid, lane);
        }
        if (m == EP2 - 1) flag_req<125>(FLAG(FL_H + pm * 32 + (lane & 15)));
        if (m == EP2) {
          wait_vm<3>();
          if (!__all(ard<125>() == tag)) poll(FLAG(FL_H + pm * 32), 16, tag);
          asm volatile("" ::: "memory");
          po.prologue<1, true>();
        }
      }
    }
    f32x4 acc[4][4];
    unsigned* fh2 = FLAG(FL_H + pm * 32 + 16);
    unsigned* fy = FLAG(FL_Y + tile * 2);
    auto yreq = [&]() {
      static_for<0, 16>([&](auto I) { constexpr int i = I.value;
        bld4_dev<128 + 4 * i, (i & 3) * 1024>(ry, lane * 16, (i >> 3) * 65536 + half * 32768 + wid * 8192 + (i & 4) * 1024); });
    };
    gemm_loop<GO, 56>(po, lane, acc, ZeroInit(), [&](int it) {
      if (it == 14) signal(FLAG(FL_H + pm1 * 32 + 16 + pn1), tag);
      if (it == 13) flag_req<125>(fh2 + (lane & 15) * FS);
      if (it == 12) { if (!__all(ard<125>() == tag)) poll(fh2, 16, tag); asm volatile("" ::: "memory"); }
      if (it == 2) { flag_req<125>(fy + (lane & 1) * FS); static_for<0, 4>([&](auto N) { gld2<80 + 2 * N.value>(b2 + pn * 128 + wn * 64 + N.value * 16 + fq * 4); }); }
    });
    if (!__all(ard<125>() == tag)) poll(fy, 2, tag);
    asm volatile("" ::: "memory");
    yreq();
    static_for<0, 8>([&](auto J) { constexpr int j = J.value; gld4<16 + 4 * (j & 3) + 32 * (j >> 2), 0>(x + xo + (size_t)j * 8 * D_); });
    const u32x2 bias[4] = {ard2<80>(), ard2<82>(), ard2<84>(), ard2<86>()};
#pragma unroll
    for (int i = 0; i < 16; ++i) { const f32x4 t = acc[i >> 2][i & 3] + bf4(bias[i & 3]);
      const f32x2 a = silu2((f32x2){t[0], t[1]}), b = silu2((f32x2){t[2], t[3]}); acc[i >> 2][i & 3] = (f32x4){a.x, a.y, b.x, b.y}; pinacc(acc[i >> 2][i & 3]); }
    wait_vm<0>(); bar();
    const u32x4 xr[8] = {ard4<16>(), ard4<20>(), ard4<24>(), ard4<28>(), ard4<48>(), ard4<52>(), ard4<56>(), ard4<60>()};
    u32x4 yr[8], yi[8];
    static_for<0, 8>([&](auto I) { yr[I.value] = ard4<128 + 4 * I.value>(); yi[I.value] = ard4<160 + 4 * I.value>(); });
    LAS unsigned char* tl = (LAS unsigned char*)shm + wid * (64 * 144);
#pragma unroll
    for (int i = 0; i < 8; ++i) {
      unsigned r[4];
#pragma unroll
      for (int e = 0; e < 4; ++e) { const f32x4 g = acc[i >> 1][(i & 1) * 2 + (e >> 1)];
        r[e] = cvt_pk_bf16((bf_lo(yr[i][e]) - bf_lo(yi[i][e])) * g[(e & 1) * 2], (bf_hi(yr[i][e]) - bf_hi(yi[i][e])) * g[(e & 1) * 2 + 1]); }
      LAS unsigned char* q = tl + ((i >> 1) * 16 + fr) * 144 + (i & 1) * 64 + fq * 8;
      *(LAS u32x2*)q = (u32x2){r[0], r[1]}; *(LAS u32x2*)(q + 32) = (u32x2){r[2], r[3]};
    }
    wait_lgkm0();
#pragma unroll
    for (int j = 0; j < 8; ++j) {
      const u32x4 r = *(const LAS u32x4*)(tl + (j * 8 + (lane >> 3)) * 144 + (lane & 7) * 16);
      u32x4 o;
#pragma unroll
      for (int e = 0; e < 4; ++e) o[e] = cvt_pk_bf16(bf_lo(xr[j][e]) + bf_lo(r[e]), bf_hi(xr[j][e]) + bf_hi(r[e]));
      __builtin_nontemporal_store(o, (u32x4*)(out + xo + (size_t)j * 8 * D_));
    }
    asm volatile(".Lkm_endo:" ::: "memory");
    if (blockIdx.x == 255) {
      poll(FLAG(FL_X + wid * 64), 64, tag);
      asm volatile("" ::: "memory"); bar();
      signal(FLAG(FL_E), tag);
    }
  }
}

extern "C" void kernel_launch(void* const* d_in, const int* in_sizes, int n_in, void* d_out, int out_size, void* d_ws, size_t ws_size, hipStream_t stream) {
  const u16* x = (const u16*)d_in[0]; const u16* ws_re = (const u16*)d_in[1]; const u16* ws_im = (const u16*)d_in[2];
  const u16* wd_re = (const u16*)d_in[3]; const u16* wd_im = (const u16*)d_in[4];
  const u16* w1 = (const u16*)d_in[6]; const u16* b1 = (const u16*)d_in[7]; const u16* w2 = (const u16*)d_in[8]; const u16* b2 = (const u16*)d_in[9];
  unsigned char* ws = (unsigned char*)d_ws;
  (void)hipFuncSetAttribute((const void*)k_main, hipFuncAttributeMaxDynamicSharedMemorySize, LDSM);
  k_main<<<256, 256, LDSM, stream>>>(x, ws_re, ws_im, wd_re, wd_im, w1, b1, w2, b2, ws, (u16*)d_out);
}
```
